```python
import torch

def _softmax(x, axis=-1):
    return torch.softmax(x, dim=axis)
def _gelu(x):
    return torch.nn.functional.gelu(x, approximate="tanh")
def _silu(x):
    return torch.nn.functional.silu(x)
def _sigmoid(x):
    return torch.sigmoid(x)
def _layernorm(x, axis=-1, eps=1e-5):
    m = x.mean(dim=axis, keepdim=True)
    v = x.var(dim=axis, keepdim=True, unbiased=False)
    return (x - m) / torch.sqrt(v + eps)
def _scatter_add(tgt, idx, src, axis=0):
    return tgt.clone().index_add_(axis, idx, src)
def _segment_sum(src, seg, n):
    out = torch.zeros((n,) + src.shape[1:], dtype=src.dtype)
    return out.index_add_(0, seg, src)
def _pad(x, pw):
    flat = tuple(v for pair in reversed(pw) for v in pair)
    return torch.nn.functional.pad(x, flat)

kernel_name = 'interleave_layers__grouped_kv_mqa__dense_activation_mish__prompt'

def setup_inputs():

    _w = (lambda a: (lambda t: t.to(torch.bfloat16) if t.is_floating_point() else t)(torch.as_tensor(a)))
    def _A_setup():
        B, S, D, H = 1, 2048, 640, 32
        Dh = D // H
        return {
            "x": _w((lambda *s: torch.randn(*s, dtype=torch.bfloat16) * 0.05)(B, S, D)),
            "wq": _w((lambda *s: torch.randn(*s, dtype=torch.bfloat16) * 0.05)(D, H * Dh)),
            "wk": _w((lambda *s: torch.randn(*s, dtype=torch.bfloat16) * 0.05)(D, Dh)),
            "wv": _w((lambda *s: torch.randn(*s, dtype=torch.bfloat16) * 0.05)(D, Dh)),
            "wo": _w((lambda *s: torch.randn(*s, dtype=torch.bfloat16) * 0.05)(H * Dh, D)),
        }
    _A_in = _A_setup()

    def _B_setup():
        B, S, D, F = 1, 2048, 640, 1728
        return {
            "x": _w((lambda *s: torch.randn(*s, dtype=torch.bfloat16) * 0.05)(B, S, D)),
            "w1": _w((lambda *s: torch.randn(*s, dtype=torch.bfloat16) * 1.6)(D, F) * (D ** -0.5)),
            "b1": _w((lambda *s: torch.randn(*s, dtype=torch.bfloat16) * 0.2)(F)),
            "w2": _w((lambda *s: torch.randn(*s, dtype=torch.bfloat16) * 0.2)(F, D) * (F ** -0.5)),
        }
    _B_in = _B_setup()

    return {
        "A_x": _A_in["x"],
        "A_wq": _A_in["wq"],
        "A_wk": _A_in["wk"],
        "A_wv": _A_in["wv"],
        "A_wo": _A_in["wo"],
        "B_x": _B_in["x"],
        "B_w1": _B_in["w1"],
        "B_b1": _B_in["b1"],
        "B_w2": _B_in["w2"],
    }

def reference(A_x, A_wq, A_wk, A_wv, A_wo, B_x, B_w1, B_b1, B_w2):
    def _A_ref(x, wq, wk, wv, wo):
        B, S, D = x.shape
        H = 32
        Dh = D // H
        q = (lambda x, *d: x.permute(*d))((x @ wq).reshape(B, S, H, Dh), 0, 2, 1, 3)
        k = x @ wk
        v = x @ wv
        scores = torch.einsum("bhqd,bkd->bhqk", q, k) * (Dh ** -0.5)
        p = _softmax(scores, axis=-1)
        o = torch.einsum("bhqk,bkd->bhqd", p, v)
        return (lambda x, *d: x.permute(*d))(o, 0, 2, 1, 3).reshape(B, S, H * Dh) @ wo

    def _B_ref(x, w1, b1, w2):
        h = torch.einsum("bsd,df->bsf", x, w1) + b1
        sp = torch.log1p(torch.exp(-torch.abs(h))) + torch.where(h > 0, h, torch.zeros_like(h))
        act = h * torch.tanh(sp)
        return torch.einsum("bsf,fd->bsd", act, w2)

    x = A_x
    x = x + _A_ref(_layernorm(x, axis=-1), A_wq, A_wk, A_wv, A_wo)
    x = x + _B_ref(_layernorm(x, axis=-1), B_w1, B_b1, B_w2)
    x = x + _A_ref(_layernorm(x, axis=-1), A_wq, A_wk, A_wv, A_wo)
    x = x + _B_ref(_layernorm(x, axis=-1), B_w1, B_b1, B_w2)
    return x
```

```cpp
#include <hip/hip_runtime.h>
#include <hip/hip_bf16.h>
#include <stdint.h>

namespace {

typedef float f32x32 __attribute__((ext_vector_type(32)));
typedef float f32x16 __attribute__((ext_vector_type(16)));
typedef float f32x8 __attribute__((ext_vector_type(8)));
typedef float f32x4 __attribute__((ext_vector_type(4)));
typedef float f32x2 __attribute__((ext_vector_type(2)));
typedef __bf16 bf16x2 __attribute__((ext_vector_type(2)));
typedef __bf16 bf16x8 __attribute__((ext_vector_type(8)));
typedef short s16x4 __attribute__((ext_vector_type(4)));
typedef unsigned u32x4 __attribute__((ext_vector_type(4)));
typedef unsigned u32x2 __attribute__((ext_vector_type(2)));
typedef unsigned short u16;
#define LAS __attribute__((address_space(3)))
#define GAS __attribute__((address_space(1)))
#define DI __device__ __forceinline__
typedef GAS unsigned gu32;
#define RLX_AGENT __ATOMIC_RELAXED, __HIP_MEMORY_SCOPE_AGENT

constexpr int SEQ = 2048, DM = 640, FF = 1728;
constexpr int ACT_SL = 2560, ACT_MB = 8 * ACT_SL, ACT_G = 4 * ACT_MB;
constexpr int ACT_BYTES = 32 * ACT_G;
constexpr int TILEB = 2624, K1OFF = 1024, V0OFF = 1280, VSTEP = 672, HALFB = 73728;
constexpr int KV_BYTES = 64 * TILEB + 4096;
constexpr int H_MB = 54 * 1024, H_BYTES = 128 * H_MB;
constexpr int WQ_J = 20 * 6 * 1024, WO_J = 20 * 5 * 1024, W1_B = 20 * 2 * 1024, W2_J = 27 * 10 * 1024;

constexpr int FLAG_STRIDE = 128;
constexpr int OFF_FLAGS = 0;
constexpr int OFF_TMO = 10 * 32 * FLAG_STRIDE;
constexpr int OFF_KVC = 11 * 32 * FLAG_STRIDE;
constexpr int FLAGS_BYTES = OFF_KVC + 2 * 64 * FLAG_STRIDE;
constexpr int OFF_XT = 65536;
constexpr int OFF_WQ = OFF_XT + ACT_BYTES;
constexpr int OFF_WO = OFF_WQ + 8 * WQ_J;
constexpr int OFF_W1 = OFF_WO + 8 * WO_J;
constexpr int OFF_W2 = OFF_W1 + 54 * W1_B;
constexpr int OFF_CSQ = OFF_W2 + 8 * W2_J;
constexpr int OFF_CS1 = OFF_CSQ + 16384;
constexpr int OFF_B1F = OFF_CS1 + 32768;
constexpr int OFF_LAYER = OFF_B1F + 8192;
constexpr int L_KV = 0, L_O = L_KV + KV_BYTES, L_X1 = L_O + ACT_BYTES, L_H = L_X1 + ACT_BYTES, L_X2 = L_H + H_BYTES, LAYER_BYTES = L_X2 + ACT_BYTES;
constexpr int WS_USED = OFF_LAYER + LAYER_BYTES + L_X2;
static_assert(KV_BYTES % 128 == 0 && LAYER_BYTES % 128 == 0 && OFF_LAYER % 128 == 0, "alignment");
enum { HOP_KV = 0, HOP_O = 1, HOP_X1 = 2, HOP_H = 3, HOP_X2 = 4 };

constexpr float QSCALE = 0.22360679774997896f * 1.4426950408889634f;
constexpr float LOG2E = 1.4426950408889634f, LN2 = 0.6931471805599453f;
constexpr float LN_EPS = 1e-5f;
constexpr unsigned SPIN_CAP = 1u << 17;

struct Params {
  const u16 *x, *wq, *wk, *wv, *wo, *w1, *b1, *w2;
  char* ws;
  u16* out;
};

DI float bf2f(unsigned b) { return __uint_as_float(b << 16); }
DI float bflo(unsigned w) { return __uint_as_float(w << 16); }
DI float bfhi(unsigned w) { return __uint_as_float(w & 0xffff0000u); }
DI unsigned cvtpk(float lo, float hi) { f32x2 v = {lo, hi}; bf16x2 b = __builtin_convertvector(v, bf16x2); return __builtin_bit_cast(unsigned, b); }
DI f32x4 mfma16(u32x4 a, u32x4 b, f32x4 c) {
  return __builtin_amdgcn_mfma_f32_16x16x32_bf16(__builtin_bit_cast(bf16x8, a), __builtin_bit_cast(bf16x8, b), c, 0, 0, 0);
}
DI f32x16 mfma32(u32x4 a, u32x4 b, f32x16 c) {
  return __builtin_amdgcn_mfma_f32_32x32x16_bf16(__builtin_bit_cast(bf16x8, a), __builtin_bit_cast(bf16x8, b), c, 0, 0, 0);
}
DI float sum_q4(float x) {
  auto a = __builtin_amdgcn_permlane16_swap(__float_as_uint(x), __float_as_uint(x), false, false);
  x = __uint_as_float(a[0]) + __uint_as_float(a[1]);
  auto b = __builtin_amdgcn_permlane32_swap(__float_as_uint(x), __float_as_uint(x), false, false);
  return __uint_as_float(b[0]) + __uint_as_float(b[1]);
}
typedef __amdgpu_buffer_rsrc_t rsrc_t;
constexpr int SC1 = 16;
constexpr int PST = 16;
template <int AUX> DI u32x4 ld128(rsrc_t rs, unsigned voff, unsigned soff) { return __builtin_amdgcn_raw_buffer_load_b128(rs, voff, soff, AUX); }
template <int AUX> DI u32x2 ld64(rsrc_t rs, unsigned voff, unsigned soff) { return __builtin_amdgcn_raw_buffer_load_b64(rs, voff, soff, AUX); }
template <int AUX> DI void st128(rsrc_t rs, u32x4 v, unsigned voff, unsigned soff) { __builtin_amdgcn_raw_buffer_store_b128(v, rs, voff + soff, 0, AUX); }
template <int AUX> DI void st64(rsrc_t rs, u32x2 v, unsigned voff, unsigned soff) { __builtin_amdgcn_raw_buffer_store_b64(v, rs, voff, soff, AUX); }
DI u32x4 lds128(LAS const char* p) { return *(LAS const u32x4*)p; }
DI u32x2 lds64(LAS const char* p) { return *(LAS const u32x2*)p; }
DI f32x4 lds128f(LAS const char* p) { return *(LAS const f32x4*)p; }
#define SB() __builtin_amdgcn_sched_barrier(0)
#define BAR() asm volatile("s_waitcnt lgkmcnt(0)\n\ts_barrier" ::: "memory")
#define WAIT_VM(n) asm volatile("s_waitcnt vmcnt(" #n ")" ::: "memory")
#define WAIT_VM_BAR(n) asm volatile("s_waitcnt vmcnt(" #n ") lgkmcnt(0)\n\ts_barrier" ::: "memory")

#define DMA_HEAD "s_mov_b32 %0, m0\n\ts_mov_b32 m0, %3\n\ts_nop 0\n\t"
#define DMA_TAIL "s_mov_b32 m0, %0"
#define DMA_OPS : "=&s"(sv) : "v"(voff), "s"(gbase), "s"(ldst) : "memory"
template <int N, bool COH> DI void dma(const char* gbase, unsigned voff, unsigned ldst) {
  unsigned sv;
  if constexpr (COH) {
    if constexpr (N == 1) asm volatile(DMA_HEAD "global_load_lds_dwordx4 %1, %2 sc1\n\t" DMA_TAIL DMA_OPS);
    if constexpr (N == 2) asm volatile(DMA_HEAD "global_load_lds_dwordx4 %1, %2 sc1\n\tglobal_load_lds_dwordx4 %1, %2 offset:1024 sc1\n\t" DMA_TAIL DMA_OPS);
    if constexpr (N == 3) asm volatile(DMA_HEAD "global_load_lds_dwordx4 %1, %2 sc1\n\tglobal_load_lds_dwordx4 %1, %2 offset:1024 sc1\n\tglobal_load_lds_dwordx4 %1, %2 offset:2048 sc1\n\t" DMA_TAIL DMA_OPS);
    if constexpr (N == 4) asm volatile(DMA_HEAD "global_load_lds_dwordx4 %1, %2 sc1\n\tglobal_load_lds_dwordx4 %1, %2 offset:1024 sc1\n\tglobal_load_lds_dwordx4 %1, %2 offset:2048 sc1\n\tglobal_load_lds_dwordx4 %1, %2 offset:3072 sc1\n\t" DMA_TAIL DMA_OPS);
  } else {
    if constexpr (N == 1) asm volatile(DMA_HEAD "global_load_lds_dwordx4 %1, %2\n\t" DMA_TAIL DMA_OPS);
    if constexpr (N == 2) asm volatile(DMA_HEAD "global_load_lds_dwordx4 %1, %2\n\tglobal_load_lds_dwordx4 %1, %2 offset:1024\n\t" DMA_TAIL DMA_OPS);
    if constexpr (N == 3) asm volatile(DMA_HEAD "global_load_lds_dwordx4 %1, %2\n\tglobal_load_lds_dwordx4 %1, %2 offset:1024\n\tglobal_load_lds_dwordx4 %1, %2 offset:2048\n\t" DMA_TAIL DMA_OPS);
    if constexpr (N == 4) asm volatile(DMA_HEAD "global_load_lds_dwordx4 %1, %2\n\tglobal_load_lds_dwordx4 %1, %2 offset:1024\n\tglobal_load_lds_dwordx4 %1, %2 offset:2048\n\tglobal_load_lds_dwordx4 %1, %2 offset:3072\n\t" DMA_TAIL DMA_OPS);
  }
}
DI void touch8k(rsrc_t rs, unsigned voff128, unsigned soff, unsigned lscratch) {
  unsigned sv;
  asm volatile("s_mov_b32 %0, m0\n\ts_mov_b32 m0, %4\n\ts_nop 0\n\tbuffer_load_dword %1, %2, %3 offen lds\n\ts_mov_b32 m0, %0"
               : "=&s"(sv) : "v"(voff128), "s"(rs), "s"(soff), "s"(lscratch) : "memory");
}
template <int NP, bool COH> DI void dma_run(const char* gbase, unsigned voff, unsigned ldst) {
#pragma unroll
  for (int i = 0; i + 4 <= NP; i += 4) dma<4, COH>(gbase + i * 1024, voff, ldst + i * 1024);
  if constexpr (NP % 4 != 0) dma<NP % 4, COH>(gbase + (NP / 4) * 4096, voff, ldst + (NP / 4) * 4096);
}

constexpr unsigned ONES2 = 0x3f803f80u;
DI float diag_bcast(const f32x4& d, int lane) {
  const int i = lane & 3;
  const float v = i == 0 ? d[0] : i == 1 ? d[1] : i == 2 ? d[2] : d[3];
  const int t = lane & 15;
  return __shfl(v, t + 16 * (t >> 2));
}
DI void ln_stats(float s, float ss, float& mean, float& rstd) {
  mean = s * (1.0f / DM);
  const float var = fmaxf(ss * (1.0f / DM) - mean * mean, 0.f);
  rstd = __builtin_amdgcn_rsqf(var + LN_EPS);
}

DI int mlp2_rot(int j) { return (27 * j) >> 3; }

constexpr int PJ_X = 0, PJ_WQ = PJ_X + 128, PJ_WO = PJ_WQ + 32, PJ_W1 = PJ_WO + 32, PJ_W2 = PJ_W1 + 56, PJ_Z = PJ_W2 + 112, PJ_END = PJ_Z + 1;
constexpr int PREP_LDS = 160 * 272 + 256;

typedef short v4i16 __attribute__((ext_vector_type(4)));
DI u32x2 tr_read(LAS const char* p) { return __builtin_bit_cast(u32x2, __builtin_amdgcn_ds_read_tr16_b64_v4i16((LAS v4i16*)p)); }
DI u32x4 tr_frag(LAS const char* lds, int pitch, int r0, int r1, int cb, int lane) {
  const int rq = (lane & 15) >> 2;
  const u32x2 lo = tr_read(lds + (r0 + rq) * pitch + cb), hi = tr_read(lds + (r1 + rq) * pitch + cb);
  return u32x4{lo[0], lo[1], hi[0], hi[1]};
}
DI float frag_sum(u32x4 v) { return ((bflo(v[0]) + bfhi(v[0])) + (bflo(v[1]) + bfhi(v[1]))) + ((bflo(v[2]) + bfhi(v[2])) + (bflo(v[3]) + bfhi(v[3]))); }
DI void rows_model(int sub, int q4, int& r0, int& r1) {
  if (sub < 4) { r0 = 80 * (sub >> 1) + 32 * (sub & 1) + 4 * q4; r1 = r0 + 16; }
  else { r0 = 64 + 4 * q4; r1 = r0 + 80; }
}
DI void rows_attn(int sub, int q4, int& r0, int& r1) {
  if (sub < 4) { r0 = 80 * (sub >> 1) + 20 * (2 * (sub & 1) + (q4 & 1)) + 4 * (q4 >> 1); r1 = r0 + 8; }
  else { r0 = 20 * q4 + 16; r1 = r0 + 80; }
}
template <int ROWS, int PER> DI void stage(LAS char* lds, int pitch, const char* src, int src_pitch, int tid) {
  constexpr int TOTAL = ROWS * PER, N = (TOTAL + 255) / 256;
  u32x4 v[N];
#pragma unroll
  for (int k = 0; k < N; ++k) {
    int i = tid + 256 * k; if (i >= TOTAL) i = TOTAL - 1;
    const int r = i / PER, c = i - r * PER;
    v[k] = *(const u32x4*)(src + (size_t)r * src_pitch + c * 16);
  }
  SB();
#pragma unroll
  for (int k = 0; k < N; ++k) {
    int i = tid + 256 * k; if (i >= TOTAL) i = TOTAL - 1;
    const int r = i / PER, c = i - r * PER;
    *(LAS u32x4*)(lds + r * pitch + c * 16) = v[k];
  }
}

__global__ __launch_bounds__(256) void prep_kernel(Params p) {
  extern __shared__ __attribute__((aligned(16))) char lds_[];
  LAS char* lds = (LAS char*)lds_;
  const int tid = threadIdx.x, lane = tid & 63, q4 = lane >> 4, b = blockIdx.x;
  const int wv = __builtin_amdgcn_readfirstlane(tid >> 6);
  char* ws = p.ws;
  const rsrc_t rs = __builtin_amdgcn_make_buffer_rsrc((void*)ws, 0, WS_USED, 0x00020000);
  if (b < 64 && wv < 2) {
    unsigned long long pc; int off; unsigned len;
    asm volatile("s_getpc_b64 %0\n mish_prep_here:\n s_mov_b32 %1, mish_main_code_begin-mish_prep_here\n s_mov_b32 %2, mish_main_code_end-mish_main_code_begin"
                 : "=s"(pc), "=s"(off), "=s"(len));
    touch8k(__builtin_amdgcn_make_buffer_rsrc((void*)(pc + (long long)off), 0, len, 0x00020000), lane * 128, ((b >> 3) * 2 + wv) * 8192,
            (unsigned)(uintptr_t)lds_ + PREP_LDS - 256);
  }
  if (b < PJ_WQ) {
    const int row = tid >> 4, c = tid & 15;
    const char* src = (const char*)p.x + (size_t)(b * 16 + row) * 1280;
    u32x4 v[5];
#pragma unroll
    for (int i = 0; i < 5; ++i) v[i] = *(const u32x4*)(src + (c + 16 * i) * 16);
#pragma unroll
    for (int i = 0; i < 5; ++i) *(LAS u32x4*)(lds + row * 1296 + (c + 16 * i) * 16) = v[i];
    __syncthreads();
    const int tk = lane & 15;
#pragma unroll
    for (int i = 0; i < 2; ++i) {
      const int j = 2 * wv + i;
      LAS const char* r = lds + tk * 1296 + (80 * j + 4 * q4) * 2;
      const unsigned dst = OFF_XT + b * ACT_MB + j * ACT_SL;
#pragma unroll
      for (int f = 0; f < 2; ++f) {
        const u32x2 lo = lds64(r + 64 * f), hi = lds64(r + 64 * f + 32);
        st128<PST>(rs, u32x4{lo[0], lo[1], hi[0], hi[1]}, lane * 16, dst + f * 1024);
      }
      st64<PST>(rs, lds64(r + 128), lane * 8, dst + 2048);
    }
  } else if (b < PJ_WO) {
    const int jb = b - PJ_WQ, pp = jb >> 3, j = jb & 7;
    constexpr int PITCH = 208;
    u32x2 kvv[3];
    bool kvz[3];
#pragma unroll
    for (int k = 0; k < 3; ++k) {
      int i = tid + 256 * k; if (i >= 640) i = 639;
      const int r = i >> 2, c = i & 3;
      const int ncg = j < 2 ? 2 : 1;
      const u16* w = j < 3 ? p.wk : p.wv;
      const int c0 = j < 3 ? 8 * j : 4 * (j - 3);
      kvv[k] = *(const u32x2*)(w + (size_t)(160 * pp + r) * 20 + c0 + (c < ncg ? 4 * c : 0));
      kvz[k] = c >= ncg;
    }
    stage<160, 10>(lds, PITCH, (const char*)p.wq + (size_t)(160 * pp) * 1280 + 160 * j, 1280, tid);
#pragma unroll
    for (int k = 0; k < 3; ++k) {
      int i = tid + 256 * k; if (i >= 640) i = 639;
      *(LAS u32x2*)(lds + (i >> 2) * PITCH + 160 + (i & 3) * 8) = kvz[k] ? u32x2{0u, 0u} : kvv[k];
    }
    __syncthreads();
    const int cg = lane & 3;
    for (int nb = wv; nb < 6; nb += 4) {
      int col;
      if (nb < 4) col = 20 * (2 * (nb >> 1) + (cg & 1)) + 8 * (cg >> 1) + 4 * (nb & 1);
      else if (nb == 4) col = 20 * cg + 16;
      else col = 80 + 4 * cg;
      u32x4 f[5];
#pragma unroll
      for (int sub = 0; sub < 5; ++sub) { int r0, r1; rows_model(sub, q4, r0, r1); f[sub] = tr_frag(lds, PITCH, r0, r1, col * 2, lane); }
      float cs = 0.f;
#pragma unroll
      for (int sub = 0; sub < 5; ++sub) {
        const int ks = 5 * pp + sub, kh = ks >= 10, s = ks - 10 * kh;
        st128<PST>(rs, f[sub], lane * 16, OFF_WQ + j * WQ_J + kh * 61440 + (nb < 5 ? s * 5 + nb : 50 + s) * 1024);
        cs += frag_sum(f[sub]);
      }
      cs = sum_q4(cs);
      if (lane < 16) __builtin_amdgcn_raw_buffer_store_b32(__float_as_uint(cs), rs, lane * 4, OFF_CSQ + ((pp * 8 + j) * 6 + nb) * 64, PST);
    }
  } else if (b < PJ_W1) {
    const int jb = b - PJ_WO, pp = jb >> 3, j = jb & 7;
    constexpr int PITCH = 176;
    stage<160, 10>(lds, PITCH, (const char*)p.wo + (size_t)(160 * pp) * 1280 + 160 * j, 1280, tid);
    __syncthreads();
    for (int nb = wv; nb < 5; nb += 4) {
      u32x4 f[5];
#pragma unroll
      for (int sub = 0; sub < 5; ++sub) { int r0, r1; rows_attn(sub, q4, r0, r1); f[sub] = tr_frag(lds, PITCH, r0, r1, (16 * nb + 4 * (lane & 3)) * 2, lane); }
#pragma unroll
      for (int sub = 0; sub < 5; ++sub) st128<PST>(rs, f[sub], lane * 16, OFF_WO + j * WO_J + ((5 * pp + sub) * 5 + nb) * 1024);
    }
  } else if (b < PJ_W2) {
    const int jb = b - PJ_W1, pp = jb & 3, cgp = jb >> 2;
    constexpr int PITCH = 272;
    const char* src = (const char*)p.w1 + (size_t)(160 * pp) * (FF * 2) + 256 * cgp;
    const int nnb = cgp == 13 ? 4 : 8;
    if (pp == 0 && tid < 16 * nnb) __builtin_amdgcn_raw_buffer_store_b32(__float_as_uint(bf2f(p.b1[128 * cgp + tid])), rs, tid * 4, OFF_B1F + 128 * cgp * 4, PST);
    if (cgp < 13) stage<160, 16>(lds, PITCH, src, FF * 2, tid); else stage<160, 8>(lds, PITCH, src, FF * 2, tid);
    __syncthreads();
    for (int nbl = wv; nbl < nnb; nbl += 4) {
      u32x4 f[5];
#pragma unroll
      for (int sub = 0; sub < 5; ++sub) { int r0, r1; rows_model(sub, q4, r0, r1); f[sub] = tr_frag(lds, PITCH, r0, r1, (16 * nbl + 4 * (lane & 3)) * 2, lane); }
      float cs = 0.f;
#pragma unroll
      for (int sub = 0; sub < 5; ++sub) {
        st128<PST>(rs, f[sub], lane * 16, OFF_W1 + (4 * cgp + (nbl >> 1)) * W1_B + ((5 * pp + sub) * 2 + (nbl & 1)) * 1024);
        cs += frag_sum(f[sub]);
      }
      cs = sum_q4(cs);
      if (lane < 16) __builtin_amdgcn_raw_buffer_store_b32(__float_as_uint(cs), rs, lane * 4, OFF_CS1 + (pp * FF + 128 * cgp + 16 * nbl) * 4, PST);
    }
  } else if (b < PJ_Z) {
    const int jb = b - PJ_W2, j = jb & 7, bq = jb >> 3;
    constexpr int PITCH = 176;
    const char* src = (const char*)p.w2 + (size_t)(128 * bq) * 1280 + 160 * j;
    if (bq < 13) stage<128, 10>(lds, PITCH, src, 1280, tid); else stage<64, 10>(lds, PITCH, src, 1280, tid);
    const int nblk = bq == 13 ? 2 : 4;
    __syncthreads();
    if (wv < nblk) {
      const int blk = 4 * bq + wv, kh = blk >= 27;
      int s = blk - 27 * kh - mlp2_rot(j); if (s < 0) s += 27;
      const int r0 = 32 * wv + 4 * q4;
      u32x4 f[5];
#pragma unroll
      for (int nb = 0; nb < 5; ++nb) f[nb] = tr_frag(lds, PITCH, r0, r0 + 16, (16 * nb + 4 * (lane & 3)) * 2, lane);
#pragma unroll
      for (int nb = 0; nb < 5; ++nb) st128<PST>(rs, f[nb], lane * 16, OFF_W2 + j * W2_J + ((s * 2 + kh) * 5 + nb) * 1024);
    }
  } else {
    for (int i = tid; i < FLAGS_BYTES / 16; i += 256) st128<PST>(rs, u32x4{0u, 0u, 0u, 0u}, i * 16, OFF_FLAGS);
    for (int i = tid; i < 2 * 64 * 4; i += 256) {
      const int L = i >> 8, t = (i >> 2) & 63, s = (i >> 1) & 1, h = i & 1;
      st128<PST>(rs, u32x4{0x3f803f80u, 0x3f803f80u, 0x3f803f80u, 0x3f803f80u},
                 L * LAYER_BYTES + L_KV + t * TILEB + V0OFF + s * VSTEP + (h * 21 + 20) * 16, OFF_LAYER);
    }
  }
}

#define ATT_PRO \
  "ds_read_b128 v[200:203], %[pk0]\n" \
  "ds_read_b64 v[204:205], %[pk1] offset:1024\n" \
  "s_waitcnt lgkmcnt(0)\n" \
  "v_mfma_f32_32x32x16_bf16 v[128:143], v[200:203], %[q0a], 0\n" \
  "v_mfma_f32_32x32x16_bf16 v[144:159], v[200:203], %[q0b], 0\n" \
  "s_nop 9\n" \
  "v_mfma_f32_32x32x4_2b_bf16 v[128:159], v[204:205], %[q1], v[128:159]\n" \
  "s_nop 15\n" \
  "s_nop 3\n"
#define ATT_SEG0 \
  "ds_read_b128 v[200:203], %[pk0] offset:2624\n" \
  "ds_read_b64 v[204:205], %[pk1] offset:3648\n" \
  "ds_read_b128 v[206:209], %[pv] offset:1280\n" \
  "ds_read_b128 v[210:213], %[pv] offset:1952\n" \
  "v_exp_f32 v128, v128\n" \
  "v_exp_f32 v129, v129\n" \
  "v_exp_f32 v130, v130\n" \
  "v_exp_f32 v131, v131\n" \
  "v_exp_f32 v132, v132\n" \
  "v_exp_f32 v133, v133\n" \
  "v_exp_f32 v134, v134\n" \
  "v_exp_f32 v135, v135\n" \
  "v_cvt_pk_bf16_f32 v192, v128, v129\n" \
  "v_cvt_pk_bf16_f32 v193, v130, v131\n" \
  "v_cvt_pk_bf16_f32 v194, v132, v133\n" \
  "v_cvt_pk_bf16_f32 v195, v134, v135\n" \
  "s_waitcnt lgkmcnt(0)\n" \
  "v_mfma_f32_32x32x16_bf16 v[160:175], v[200:203], %[q0a], 0\n" \
  "v_exp_f32 v136, v136\n" \
  "v_exp_f32 v137, v137\n" \
  "v_mfma_f32_32x32x16_bf16 %[Oa], v[206:209], v[192:195], %[Oa]\n" \
  "s_mov_b32 m0, %[lk]\n" \
  "s_add_u32 %[lk], %[lk], 0x3000\n" \
  "buffer_load_dwordx4 %[vo], %[rs], %[sk] offen sc1 lds\n" \
  "buffer_load_dwordx4 %[vo], %[rs], %[sk] offen offset:1024 sc1 lds\n" \
  "buffer_load_dwordx4 %[vo], %[rs], %[sk] offen offset:2048 sc1 lds\n" \
  "s_add_u32 %[sk], %[sk], 0x3000\n" \
  "v_exp_f32 v138, v138\n" \
  "v_exp_f32 v139, v139\n" \
  "v_exp_f32 v140, v140\n" \
  "v_mfma_f32_32x32x16_bf16 v[176:191], v[200:203], %[q0b], 0\n" \
  "v_exp_f32 v141, v141\n" \
  "v_exp_f32 v142, v142\n" \
  "v_exp_f32 v143, v143\n" \
  "v_cvt_pk_bf16_f32 v196, v136, v137\n" \
  "v_cvt_pk_bf16_f32 v197, v138, v139\n" \
  "v_cvt_pk_bf16_f32 v198, v140, v141\n" \
  "v_cvt_pk_bf16_f32 v199, v142, v143\n" \
  "v_exp_f32 v144, v144\n" \
  "v_exp_f32 v145, v145\n" \
  "v_mfma_f32_32x32x16_bf16 %[Oa], v[210:213], v[196:199], %[Oa]\n" \
  "v_exp_f32 v146, v146\n" \
  "v_exp_f32 v147, v147\n" \
  "v_mfma_f32_32x32x4_2b_bf16 v[160:191], v[204:205], %[q1], v[160:191]\n" \
  "v_exp_f32 v148, v148\n" \
  "v_exp_f32 v149, v149\n" \
  "v_exp_f32 v150, v150\n" \
  "v_exp_f32 v151, v151\n" \
  "v_cvt_pk_bf16_f32 v192, v144, v145\n" \
  "v_cvt_pk_bf16_f32 v193, v146, v147\n" \
  "v_cvt_pk_bf16_f32 v194, v148, v149\n" \
  "v_cvt_pk_bf16_f32 v195, v150, v151\n" \
  "v_exp_f32 v152, v152\n" \
  "v_exp_f32 v153, v153\n" \
  "v_mfma_f32_32x32x16_bf16 %[Ob], v[206:209], v[192:195], %[Ob]\n" \
  "v_exp_f32 v154, v154\n" \
  "v_exp_f32 v155, v155\n" \
  "v_exp_f32 v156, v156\n" \
  "v_exp_f32 v157, v157\n" \
  "v_exp_f32 v158, v158\n" \
  "v_exp_f32 v159, v159\n" \
  "v_cvt_pk_bf16_f32 v196, v152, v153\n" \
  "v_cvt_pk_bf16_f32 v197, v154, v155\n" \
  "v_cvt_pk_bf16_f32 v198, v156, v157\n" \
  "v_cvt_pk_bf16_f32 v199, v158, v159\n" \
  "ds_read_b128 v[200:203], %[pk0] offset:5248\n" \
  "ds_read_b64 v[204:205], %[pk1] offset:6272\n" \
  "v_mfma_f32_32x32x16_bf16 %[Ob], v[210:213], v[196:199], %[Ob]\n" \
  "ds_read_b128 v[206:209], %[pv] offset:3904\n" \
  "ds_read_b128 v[210:213], %[pv] offset:4576\n" \
  "v_exp_f32 v160, v160\n" \
  "v_exp_f32 v161, v161\n" \
  "v_exp_f32 v162, v162\n" \
  "v_exp_f32 v163, v163\n" \
  "v_exp_f32 v164, v164\n" \
  "v_exp_f32 v165, v165\n" \
  "v_exp_f32 v166, v166\n" \
  "v_exp_f32 v167, v167\n" \
  "v_cvt_pk_bf16_f32 v192, v160, v161\n" \
  "v_cvt_pk_bf16_f32 v193, v162, v163\n" \
  "v_cvt_pk_bf16_f32 v194, v164, v165\n" \
  "v_cvt_pk_bf16_f32 v195, v166, v167\n" \
  "s_waitcnt lgkmcnt(0)\n" \
  "v_mfma_f32_32x32x16_bf16 v[128:143], v[200:203], %[q0a], 0\n" \
  "v_exp_f32 v168, v168\n" \
  "v_exp_f32 v169, v169\n" \
  "v_mfma_f32_32x32x16_bf16 %[Oa], v[206:209], v[192:195], %[Oa]\n" \
  "s_mov_b32 m0, %[lk]\n" \
  "s_add_u32 %[lk], %[lk], 0x3000\n" \
  "buffer_load_dwordx4 %[vo], %[rs], %[sk] offen sc1 lds\n" \
  "buffer_load_dwordx4 %[vo], %[rs], %[sk] offen offset:1024 sc1 lds\n" \
  "buffer_load_dwordx4 %[vo], %[rs], %[sk] offen offset:2048 sc1 lds\n" \
  "s_add_u32 %[sk], %[sk], 0x3000\n" \
  "v_exp_f32 v170, v170\n" \
  "v_exp_f32 v171, v171\n" \
  "v_exp_f32 v172, v172\n" \
  "v_mfma_f32_32x32x16_bf16 v[144:159], v[200:203], %[q0b], 0\n" \
  "v_exp_f32 v173, v173\n" \
  "v_exp_f32 v174, v174\n" \
  "v_exp_f32 v175, v175\n" \
  "v_cvt_pk_bf16_f32 v196, v168, v169\n" \
  "v_cvt_pk_bf16_f32 v197, v170, v171\n" \
  "v_cvt_pk_bf16_f32 v198, v172, v173\n" \
  "v_cvt_pk_bf16_f32 v199, v174, v175\n" \
  "v_exp_f32 v176, v176\n" \
  "v_exp_f32 v177, v177\n" \
  "v_mfma_f32_32x32x16_bf16 %[Oa], v[210:213], v[196:199], %[Oa]\n" \
  "v_exp_f32 v178, v178\n" \
  "v_exp_f32 v179, v179\n" \
  "v_mfma_f32_32x32x4_2b_bf16 v[128:159], v[204:205], %[q1], v[128:159]\n" \
  "v_exp_f32 v180, v180\n" \
  "v_exp_f32 v181, v181\n" \
  "v_exp_f32 v182, v182\n" \
  "v_exp_f32 v183, v183\n" \
  "v_cvt_pk_bf16_f32 v192, v176, v177\n" \
  "v_cvt_pk_bf16_f32 v193, v178, v179\n" \
  "v_cvt_pk_bf16_f32 v194, v180, v181\n" \
  "v_cvt_pk_bf16_f32 v195, v182, v183\n" \
  "v_exp_f32 v184, v184\n" \
  "v_exp_f32 v185, v185\n" \
  "v_mfma_f32_32x32x16_bf16 %[Ob], v[206:209], v[192:195], %[Ob]\n" \
  "v_exp_f32 v186, v186\n" \
  "v_exp_f32 v187, v187\n" \
  "v_exp_f32 v188, v188\n" \
  "v_exp_f32 v189, v189\n" \
  "v_exp_f32 v190, v190\n" \
  "v_exp_f32 v191, v191\n" \
  "v_cvt_pk_bf16_f32 v196, v184, v185\n" \
  "v_cvt_pk_bf16_f32 v197, v186, v187\n" \
  "v_cvt_pk_bf16_f32 v198, v188, v189\n" \
  "v_cvt_pk_bf16_f32 v199, v190, v191\n" \
  "ds_read_b128 v[200:203], %[pk0] offset:7872\n" \
  "ds_read_b64 v[204:205], %[pk1] offset:8896\n" \
  "v_mfma_f32_32x32x16_bf16 %[Ob], v[210:213], v[196:199], %[Ob]\n" \
  "ds_read_b128 v[206:209], %[pv] offset:6528\n" \
  "ds_read_b128 v[210:213], %[pv] offset:7200\n" \
  "v_exp_f32 v128, v128\n" \
  "v_exp_f32 v129, v129\n" \
  "v_exp_f32 v130, v130\n" \
  "v_exp_f32 v131, v131\n" \
  "v_exp_f32 v132, v132\n" \
  "v_exp_f32 v133, v133\n" \
  "v_exp_f32 v134, v134\n" \
  "v_exp_f32 v135, v135\n" \
  "v_cvt_pk_bf16_f32 v192, v128, v129\n" \
  "v_cvt_pk_bf16_f32 v193, v130, v131\n" \
  "v_cvt_pk_bf16_f32 v194, v132, v133\n" \
  "v_cvt_pk_bf16_f32 v195, v134, v135\n" \
  "s_waitcnt lgkmcnt(0)\n" \
  "v_mfma_f32_32x32x16_bf16 v[160:175], v[200:203], %[q0a], 0\n" \
  "v_exp_f32 v136, v136\n" \
  "v_exp_f32 v137, v137\n" \
  "v_mfma_f32_32x32x16_bf16 %[Oa], v[206:209], v[192:195], %[Oa]\n" \
  "s_mov_b32 m0, %[lk]\n" \
  "s_add_u32 %[lk], %[lk], 0x3000\n" \
  "buffer_load_dwordx4 %[vo], %[rs], %[sk] offen sc1 lds\n" \
  "buffer_load_dwordx4 %[vo], %[rs], %[sk] offen offset:1024 sc1 lds\n" \
  "buffer_load_dwordx4 %[vo], %[rs], %[sk] offen offset:2048 sc1 lds\n" \
  "s_add_u32 %[sk], %[sk], 0x3000\n" \
  "v_exp_f32 v138, v138\n" \
  "v_exp_f32 v139, v139\n" \
  "v_exp_f32 v140, v140\n" \
  "v_mfma_f32_32x32x16_bf16 v[176:191], v[200:203], %[q0b], 0\n" \
  "v_exp_f32 v141, v141\n" \
  "v_exp_f32 v142, v142\n" \
  "v_exp_f32 v143, v143\n" \
  "v_cvt_pk_bf16_f32 v196, v136, v137\n" \
  "v_cvt_pk_bf16_f32 v197, v138, v139\n" \
  "v_cvt_pk_bf16_f32 v198, v140, v141\n" \
  "v_cvt_pk_bf16_f32 v199, v142, v143\n" \
  "v_exp_f32 v144, v144\n" \
  "v_exp_f32 v145, v145\n" \
  "v_mfma_f32_32x32x16_bf16 %[Oa], v[210:213], v[196:199], %[Oa]\n" \
  "v_exp_f32 v146, v146\n" \
  "v_exp_f32 v147, v147\n" \
  "v_mfma_f32_32x32x4_2b_bf16 v[160:191], v[204:205], %[q1], v[160:191]\n" \
  "v_exp_f32 v148, v148\n" \
  "v_exp_f32 v149, v149\n" \
  "v_exp_f32 v150, v150\n" \
  "v_exp_f32 v151, v151\n" \
  "v_cvt_pk_bf16_f32 v192, v144, v145\n" \
  "v_cvt_pk_bf16_f32 v193, v146, v147\n" \
  "v_cvt_pk_bf16_f32 v194, v148, v149\n" \
  "v_cvt_pk_bf16_f32 v195, v150, v151\n" \
  "v_exp_f32 v152, v152\n" \
  "v_exp_f32 v153, v153\n" \
  "v_mfma_f32_32x32x16_bf16 %[Ob], v[206:209], v[192:195], %[Ob]\n" \
  "v_exp_f32 v154, v154\n" \
  "v_exp_f32 v155, v155\n" \
  "v_exp_f32 v156, v156\n" \
  "v_exp_f32 v157, v157\n" \
  "v_exp_f32 v158, v158\n" \
  "v_exp_f32 v159, v159\n" \
  "v_cvt_pk_bf16_f32 v196, v152, v153\n" \
  "v_cvt_pk_bf16_f32 v197, v154, v155\n" \
  "v_cvt_pk_bf16_f32 v198, v156, v157\n" \
  "v_cvt_pk_bf16_f32 v199, v158, v159\n"
#define ATT_SEG1 \
  "ds_read_b128 v[200:203], %[pk0] offset:10496\n" \
  "ds_read_b64 v[204:205], %[pk1] offset:11520\n" \
  "v_mfma_f32_32x32x16_bf16 %[Ob], v[210:213], v[196:199], %[Ob]\n" \
  "ds_read_b128 v[206:209], %[pv] offset:9152\n" \
  "ds_read_b128 v[210:213], %[pv] offset:9824\n" \
  "v_exp_f32 v160, v160\n" \
  "v_exp_f32 v161, v161\n" \
  "v_exp_f32 v162, v162\n" \
  "v_exp_f32 v163, v163\n" \
  "v_exp_f32 v164, v164\n" \
  "v_exp_f32 v165, v165\n" \
  "v_exp_f32 v166, v166\n" \
  "v_exp_f32 v167, v167\n" \
  "v_cvt_pk_bf16_f32 v192, v160, v161\n" \
  "v_cvt_pk_bf16_f32 v193, v162, v163\n" \
  "v_cvt_pk_bf16_f32 v194, v164, v165\n" \
  "v_cvt_pk_bf16_f32 v195, v166, v167\n" \
  "s_waitcnt lgkmcnt(0)\n" \
  "v_mfma_f32_32x32x16_bf16 v[128:143], v[200:203], %[q0a], 0\n" \
  "v_exp_f32 v168, v168\n" \
  "v_exp_f32 v169, v169\n" \
  "v_mfma_f32_32x32x16_bf16 %[Oa], v[206:209], v[192:195], %[Oa]\n" \
  "s_mov_b32 m0, %[lk]\n" \
  "s_add_u32 %[lk], %[lk], 0x3000\n" \
  "buffer_load_dwordx4 %[vo], %[rs], %[sk] offen sc1 lds\n" \
  "buffer_load_dwordx4 %[vo], %[rs], %[sk] offen offset:1024 sc1 lds\n" \
  "buffer_load_dwordx4 %[vo], %[rs], %[sk] offen offset:2048 sc1 lds\n" \
  "s_add_u32 %[sk], %[sk], 0x3000\n" \
  "v_exp_f32 v170, v170\n" \
  "v_exp_f32 v171, v171\n" \
  "v_exp_f32 v172, v172\n" \
  "v_mfma_f32_32x32x16_bf16 v[144:159], v[200:203], %[q0b], 0\n" \
  "v_exp_f32 v173, v173\n" \
  "v_exp_f32 v174, v174\n" \
  "v_exp_f32 v175, v175\n" \
  "v_cvt_pk_bf16_f32 v196, v168, v169\n" \
  "v_cvt_pk_bf16_f32 v197, v170, v171\n" \
  "v_cvt_pk_bf16_f32 v198, v172, v173\n" \
  "v_cvt_pk_bf16_f32 v199, v174, v175\n" \
  "v_exp_f32 v176, v176\n" \
  "v_exp_f32 v177, v177\n" \
  "v_mfma_f32_32x32x16_bf16 %[Oa], v[210:213], v[196:199], %[Oa]\n" \
  "v_exp_f32 v178, v178\n" \
  "v_exp_f32 v179, v179\n" \
  "v_mfma_f32_32x32x4_2b_bf16 v[128:159], v[204:205], %[q1], v[128:159]\n" \
  "v_exp_f32 v180, v180\n" \
  "v_exp_f32 v181, v181\n" \
  "v_exp_f32 v182, v182\n" \
  "v_exp_f32 v183, v183\n" \
  "v_cvt_pk_bf16_f32 v192, v176, v177\n" \
  "v_cvt_pk_bf16_f32 v193, v178, v179\n" \
  "v_cvt_pk_bf16_f32 v194, v180, v181\n" \
  "v_cvt_pk_bf16_f32 v195, v182, v183\n" \
  "v_exp_f32 v184, v184\n" \
  "v_exp_f32 v185, v185\n" \
  "v_mfma_f32_32x32x16_bf16 %[Ob], v[206:209], v[192:195], %[Ob]\n" \
  "v_exp_f32 v186, v186\n" \
  "v_exp_f32 v187, v187\n" \
  "v_exp_f32 v188, v188\n" \
  "v_exp_f32 v189, v189\n" \
  "v_exp_f32 v190, v190\n" \
  "v_exp_f32 v191, v191\n" \
  "v_cvt_pk_bf16_f32 v196, v184, v185\n" \
  "v_cvt_pk_bf16_f32 v197, v186, v187\n" \
  "v_cvt_pk_bf16_f32 v198, v188, v189\n" \
  "v_cvt_pk_bf16_f32 v199, v190, v191\n" \
  "ds_read_b128 v[200:203], %[pk0] offset:13120\n" \
  "ds_read_b64 v[204:205], %[pk1] offset:14144\n" \
  "v_mfma_f32_32x32x16_bf16 %[Ob], v[210:213], v[196:199], %[Ob]\n" \
  "ds_read_b128 v[206:209], %[pv] offset:11776\n" \
  "ds_read_b128 v[210:213], %[pv] offset:12448\n" \
  "v_exp_f32 v128, v128\n" \
  "v_exp_f32 v129, v129\n" \
  "v_exp_f32 v130, v130\n" \
  "v_exp_f32 v131, v131\n" \
  "v_exp_f32 v132, v132\n" \
  "v_exp_f32 v133, v133\n" \
  "v_exp_f32 v134, v134\n" \
  "v_exp_f32 v135, v135\n" \
  "v_cvt_pk_bf16_f32 v192, v128, v129\n" \
  "v_cvt_pk_bf16_f32 v193, v130, v131\n" \
  "v_cvt_pk_bf16_f32 v194, v132, v133\n" \
  "v_cvt_pk_bf16_f32 v195, v134, v135\n" \
  "s_waitcnt lgkmcnt(0)\n" \
  "v_mfma_f32_32x32x16_bf16 v[160:175], v[200:203], %[q0a], 0\n" \
  "v_exp_f32 v136, v136\n" \
  "v_exp_f32 v137, v137\n" \
  "v_mfma_f32_32x32x16_bf16 %[Oa], v[206:209], v[192:195], %[Oa]\n" \
  "v_exp_f32 v138, v138\n" \
  "v_exp_f32 v139, v139\n" \
  "v_exp_f32 v140, v140\n" \
  "v_mfma_f32_32x32x16_bf16 v[176:191], v[200:203], %[q0b], 0\n" \
  "v_exp_f32 v141, v141\n" \
  "v_exp_f32 v142, v142\n" \
  "v_exp_f32 v143, v143\n" \
  "v_cvt_pk_bf16_f32 v196, v136, v137\n" \
  "v_cvt_pk_bf16_f32 v197, v138, v139\n" \
  "v_cvt_pk_bf16_f32 v198, v140, v141\n" \
  "v_cvt_pk_bf16_f32 v199, v142, v143\n" \
  "v_exp_f32 v144, v144\n" \
  "v_exp_f32 v145, v145\n" \
  "v_mfma_f32_32x32x16_bf16 %[Oa], v[210:213], v[196:199], %[Oa]\n" \
  "v_exp_f32 v146, v146\n" \
  "v_exp_f32 v147, v147\n" \
  "v_mfma_f32_32x32x4_2b_bf16 v[160:191], v[204:205], %[q1], v[160:191]\n" \
  "v_exp_f32 v148, v148\n" \
  "v_exp_f32 v149, v149\n" \
  "v_exp_f32 v150, v150\n" \
  "v_exp_f32 v151, v151\n" \
  "v_cvt_pk_bf16_f32 v192, v144, v145\n" \
  "v_cvt_pk_bf16_f32 v193, v146, v147\n" \
  "v_cvt_pk_bf16_f32 v194, v148, v149\n" \
  "v_cvt_pk_bf16_f32 v195, v150, v151\n" \
  "v_exp_f32 v152, v152\n" \
  "v_exp_f32 v153, v153\n" \
  "v_mfma_f32_32x32x16_bf16 %[Ob], v[206:209], v[192:195], %[Ob]\n" \
  "v_exp_f32 v154, v154\n" \
  "v_exp_f32 v155, v155\n" \
  "v_exp_f32 v156, v156\n" \
  "v_exp_f32 v157, v157\n" \
  "v_exp_f32 v158, v158\n" \
  "v_exp_f32 v159, v159\n" \
  "v_cvt_pk_bf16_f32 v196, v152, v153\n" \
  "v_cvt_pk_bf16_f32 v197, v154, v155\n" \
  "v_cvt_pk_bf16_f32 v198, v156, v157\n" \
  "v_cvt_pk_bf16_f32 v199, v158, v159\n"
#define ATT_SEG2 \
  "ds_read_b128 v[200:203], %[pk0] offset:15744\n" \
  "ds_read_b64 v[204:205], %[pk1] offset:16768\n" \
  "v_mfma_f32_32x32x16_bf16 %[Ob], v[210:213], v[196:199], %[Ob]\n" \
  "ds_read_b128 v[206:209], %[pv] offset:14400\n" \
  "ds_read_b128 v[210:213], %[pv] offset:15072\n" \
  "v_exp_f32 v160, v160\n" \
  "v_exp_f32 v161, v161\n" \
  "v_exp_f32 v162, v162\n" \
  "v_exp_f32 v163, v163\n" \
  "v_exp_f32 v164, v164\n" \
  "v_exp_f32 v165, v165\n" \
  "v_exp_f32 v166, v166\n" \
  "v_exp_f32 v167, v167\n" \
  "v_cvt_pk_bf16_f32 v192, v160, v161\n" \
  "v_cvt_pk_bf16_f32 v193, v162, v163\n" \
  "v_cvt_pk_bf16_f32 v194, v164, v165\n" \
  "v_cvt_pk_bf16_f32 v195, v166, v167\n" \
  "s_waitcnt lgkmcnt(0)\n" \
  "v_mfma_f32_32x32x16_bf16 v[128:143], v[200:203], %[q0a], 0\n" \
  "v_exp_f32 v168, v168\n" \
  "v_exp_f32 v169, v169\n" \
  "v_mfma_f32_32x32x16_bf16 %[Oa], v[206:209], v[192:195], %[Oa]\n" \
  "s_cmp_eq_u32 %[wm3], 0\n" \
  "s_cbranch_scc0 1f\n" \
  "buffer_load_dwordx4 v[32:35], %[vo], %[rw], %[so] offen\n" \
  "buffer_load_dwordx4 v[36:39], %[vo], %[rw], %[so] offen offset:1024\n" \
  "buffer_load_dwordx4 v[40:43], %[vo], %[rw], %[so] offen offset:2048\n" \
  "buffer_load_dwordx4 v[44:47], %[vo], %[rw], %[so] offen offset:3072\n" \
  "s_add_u32 %[so], %[so], 0x1000\n" \
  "1:\n" \
  "v_exp_f32 v170, v170\n" \
  "v_exp_f32 v171, v171\n" \
  "v_exp_f32 v172, v172\n" \
  "v_mfma_f32_32x32x16_bf16 v[144:159], v[200:203], %[q0b], 0\n" \
  "v_exp_f32 v173, v173\n" \
  "v_exp_f32 v174, v174\n" \
  "v_exp_f32 v175, v175\n" \
  "v_cvt_pk_bf16_f32 v196, v168, v169\n" \
  "v_cvt_pk_bf16_f32 v197, v170, v171\n" \
  "v_cvt_pk_bf16_f32 v198, v172, v173\n" \
  "v_cvt_pk_bf16_f32 v199, v174, v175\n" \
  "v_exp_f32 v176, v176\n" \
  "v_exp_f32 v177, v177\n" \
  "v_mfma_f32_32x32x16_bf16 %[Oa], v[210:213], v[196:199], %[Oa]\n" \
  "v_exp_f32 v178, v178\n" \
  "v_exp_f32 v179, v179\n" \
  "v_mfma_f32_32x32x4_2b_bf16 v[128:159], v[204:205], %[q1], v[128:159]\n" \
  "v_exp_f32 v180, v180\n" \
  "v_exp_f32 v181, v181\n" \
  "v_exp_f32 v182, v182\n" \
  "v_exp_f32 v183, v183\n" \
  "v_cvt_pk_bf16_f32 v192, v176, v177\n" \
  "v_cvt_pk_bf16_f32 v193, v178, v179\n" \
  "v_cvt_pk_bf16_f32 v194, v180, v181\n" \
  "v_cvt_pk_bf16_f32 v195, v182, v183\n" \
  "v_exp_f32 v184, v184\n" \
  "v_exp_f32 v185, v185\n" \
  "v_mfma_f32_32x32x16_bf16 %[Ob], v[206:209], v[192:195], %[Ob]\n" \
  "v_exp_f32 v186, v186\n" \
  "v_exp_f32 v187, v187\n" \
  "v_exp_f32 v188, v188\n" \
  "v_exp_f32 v189, v189\n" \
  "v_exp_f32 v190, v190\n" \
  "v_exp_f32 v191, v191\n" \
  "v_cvt_pk_bf16_f32 v196, v184, v185\n" \
  "v_cvt_pk_bf16_f32 v197, v186, v187\n" \
  "v_cvt_pk_bf16_f32 v198, v188, v189\n" \
  "v_cvt_pk_bf16_f32 v199, v190, v191\n" \
  "ds_read_b128 v[200:203], %[pk0] offset:18368\n" \
  "ds_read_b64 v[204:205], %[pk1] offset:19392\n" \
  "v_mfma_f32_32x32x16_bf16 %[Ob], v[210:213], v[196:199], %[Ob]\n" \
  "ds_read_b128 v[206:209], %[pv] offset:17024\n" \
  "ds_read_b128 v[210:213], %[pv] offset:17696\n" \
  "v_exp_f32 v128, v128\n" \
  "v_exp_f32 v129, v129\n" \
  "v_exp_f32 v130, v130\n" \
  "v_exp_f32 v131, v131\n" \
  "v_exp_f32 v132, v132\n" \
  "v_exp_f32 v133, v133\n" \
  "v_exp_f32 v134, v134\n" \
  "v_exp_f32 v135, v135\n" \
  "v_cvt_pk_bf16_f32 v192, v128, v129\n" \
  "v_cvt_pk_bf16_f32 v193, v130, v131\n" \
  "v_cvt_pk_bf16_f32 v194, v132, v133\n" \
  "v_cvt_pk_bf16_f32 v195, v134, v135\n" \
  "s_waitcnt lgkmcnt(0)\n" \
  "v_mfma_f32_32x32x16_bf16 v[160:175], v[200:203], %[q0a], 0\n" \
  "v_exp_f32 v136, v136\n" \
  "v_exp_f32 v137, v137\n" \
  "v_mfma_f32_32x32x16_bf16 %[Oa], v[206:209], v[192:195], %[Oa]\n" \
  "s_cmp_eq_u32 %[wm3], 1\n" \
  "s_cbranch_scc0 1f\n" \
  "buffer_load_dwordx4 v[32:35], %[vo], %[rw], %[so] offen\n" \
  "buffer_load_dwordx4 v[36:39], %[vo], %[rw], %[so] offen offset:1024\n" \
  "buffer_load_dwordx4 v[40:43], %[vo], %[rw], %[so] offen offset:2048\n" \
  "buffer_load_dwordx4 v[44:47], %[vo], %[rw], %[so] offen offset:3072\n" \
  "s_add_u32 %[so], %[so], 0x1000\n" \
  "1:\n" \
  "v_exp_f32 v138, v138\n" \
  "v_exp_f32 v139, v139\n" \
  "v_exp_f32 v140, v140\n" \
  "v_mfma_f32_32x32x16_bf16 v[176:191], v[200:203], %[q0b], 0\n" \
  "v_exp_f32 v141, v141\n" \
  "v_exp_f32 v142, v142\n" \
  "v_exp_f32 v143, v143\n" \
  "v_cvt_pk_bf16_f32 v196, v136, v137\n" \
  "v_cvt_pk_bf16_f32 v197, v138, v139\n" \
  "v_cvt_pk_bf16_f32 v198, v140, v141\n" \
  "v_cvt_pk_bf16_f32 v199, v142, v143\n" \
  "v_exp_f32 v144, v144\n" \
  "v_exp_f32 v145, v145\n" \
  "v_mfma_f32_32x32x16_bf16 %[Oa], v[210:213], v[196:199], %[Oa]\n" \
  "v_exp_f32 v146, v146\n" \
  "v_exp_f32 v147, v147\n" \
  "v_mfma_f32_32x32x4_2b_bf16 v[160:191], v[204:205], %[q1], v[160:191]\n" \
  "v_exp_f32 v148, v148\n" \
  "v_exp_f32 v149, v149\n" \
  "v_exp_f32 v150, v150\n" \
  "v_exp_f32 v151, v151\n" \
  "v_cvt_pk_bf16_f32 v192, v144, v145\n" \
  "v_cvt_pk_bf16_f32 v193, v146, v147\n" \
  "v_cvt_pk_bf16_f32 v194, v148, v149\n" \
  "v_cvt_pk_bf16_f32 v195, v150, v151\n" \
  "v_exp_f32 v152, v152\n" \
  "v_exp_f32 v153, v153\n" \
  "v_mfma_f32_32x32x16_bf16 %[Ob], v[206:209], v[192:195], %[Ob]\n" \
  "v_exp_f32 v154, v154\n" \
  "v_exp_f32 v155, v155\n" \
  "v_exp_f32 v156, v156\n" \
  "v_exp_f32 v157, v157\n" \
  "v_exp_f32 v158, v158\n" \
  "v_exp_f32 v159, v159\n" \
  "v_cvt_pk_bf16_f32 v196, v152, v153\n" \
  "v_cvt_pk_bf16_f32 v197, v154, v155\n" \
  "v_cvt_pk_bf16_f32 v198, v156, v157\n" \
  "v_cvt_pk_bf16_f32 v199, v158, v159\n" \
  "ds_read_b128 v[200:203], %[pk0] offset:20992\n" \
  "ds_read_b64 v[204:205], %[pk1] offset:22016\n" \
  "v_mfma_f32_32x32x16_bf16 %[Ob], v[210:213], v[196:199], %[Ob]\n" \
  "ds_read_b128 v[206:209], %[pv] offset:19648\n" \
  "ds_read_b128 v[210:213], %[pv] offset:20320\n" \
  "v_exp_f32 v160, v160\n" \
  "v_exp_f32 v161, v161\n" \
  "v_exp_f32 v162, v162\n" \
  "v_exp_f32 v163, v163\n" \
  "v_exp_f32 v164, v164\n" \
  "v_exp_f32 v165, v165\n" \
  "v_exp_f32 v166, v166\n" \
  "v_exp_f32 v167, v167\n" \
  "v_cvt_pk_bf16_f32 v192, v160, v161\n" \
  "v_cvt_pk_bf16_f32 v193, v162, v163\n" \
  "v_cvt_pk_bf16_f32 v194, v164, v165\n" \
  "v_cvt_pk_bf16_f32 v195, v166, v167\n" \
  "s_waitcnt lgkmcnt(0)\n" \
  "v_mfma_f32_32x32x16_bf16 v[128:143], v[200:203], %[q0a], 0\n" \
  "v_exp_f32 v168, v168\n" \
  "v_exp_f32 v169, v169\n" \
  "v_mfma_f32_32x32x16_bf16 %[Oa], v[206:209], v[192:195], %[Oa]\n" \
  "s_cmp_eq_u32 %[wm3], 2\n" \
  "s_cbranch_scc0 1f\n" \
  "buffer_load_dwordx4 v[32:35], %[vo], %[rw], %[so] offen\n" \
  "buffer_load_dwordx4 v[36:39], %[vo], %[rw], %[so] offen offset:1024\n" \
  "buffer_load_dwordx4 v[40:43], %[vo], %[rw], %[so] offen offset:2048\n" \
  "buffer_load_dwordx4 v[44:47], %[vo], %[rw], %[so] offen offset:3072\n" \
  "s_add_u32 %[so], %[so], 0x1000\n" \
  "1:\n" \
  "v_exp_f32 v170, v170\n" \
  "v_exp_f32 v171, v171\n" \
  "v_exp_f32 v172, v172\n" \
  "v_mfma_f32_32x32x16_bf16 v[144:159], v[200:203], %[q0b], 0\n" \
  "v_exp_f32 v173, v173\n" \
  "v_exp_f32 v174, v174\n" \
  "v_exp_f32 v175, v175\n" \
  "v_cvt_pk_bf16_f32 v196, v168, v169\n" \
  "v_cvt_pk_bf16_f32 v197, v170, v171\n" \
  "v_cvt_pk_bf16_f32 v198, v172, v173\n" \
  "v_cvt_pk_bf16_f32 v199, v174, v175\n" \
  "v_exp_f32 v176, v176\n" \
  "v_exp_f32 v177, v177\n" \
  "v_mfma_f32_32x32x16_bf16 %[Oa], v[210:213], v[196:199], %[Oa]\n" \
  "v_exp_f32 v178, v178\n" \
  "v_exp_f32 v179, v179\n" \
  "v_mfma_f32_32x32x4_2b_bf16 v[128:159], v[204:205], %[q1], v[128:159]\n" \
  "v_exp_f32 v180, v180\n" \
  "v_exp_f32 v181, v181\n" \
  "v_exp_f32 v182, v182\n" \
  "v_exp_f32 v183, v183\n" \
  "v_cvt_pk_bf16_f32 v192, v176, v177\n" \
  "v_cvt_pk_bf16_f32 v193, v178, v179\n" \
  "v_cvt_pk_bf16_f32 v194, v180, v181\n" \
  "v_cvt_pk_bf16_f32 v195, v182, v183\n" \
  "v_exp_f32 v184, v184\n" \
  "v_exp_f32 v185, v185\n" \
  "v_mfma_f32_32x32x16_bf16 %[Ob], v[206:209], v[192:195], %[Ob]\n" \
  "v_exp_f32 v186, v186\n" \
  "v_exp_f32 v187, v187\n" \
  "v_exp_f32 v188, v188\n" \
  "v_exp_f32 v189, v189\n" \
  "v_exp_f32 v190, v190\n" \
  "v_exp_f32 v191, v191\n" \
  "v_cvt_pk_bf16_f32 v196, v184, v185\n" \
  "v_cvt_pk_bf16_f32 v197, v186, v187\n" \
  "v_cvt_pk_bf16_f32 v198, v188, v189\n" \
  "v_cvt_pk_bf16_f32 v199, v190, v191\n" \
  "ds_read_b128 v[200:203], %[pk0] offset:23616\n" \
  "ds_read_b64 v[204:205], %[pk1] offset:24640\n" \
  "v_mfma_f32_32x32x16_bf16 %[Ob], v[210:213], v[196:199], %[Ob]\n" \
  "ds_read_b128 v[206:209], %[pv] offset:22272\n" \
  "ds_read_b128 v[210:213], %[pv] offset:22944\n" \
  "v_exp_f32 v128, v128\n" \
  "v_exp_f32 v129, v129\n" \
  "v_exp_f32 v130, v130\n" \
  "v_exp_f32 v131, v131\n" \
  "v_exp_f32 v132, v132\n" \
  "v_exp_f32 v133, v133\n" \
  "v_exp_f32 v134, v134\n" \
  "v_exp_f32 v135, v135\n" \
  "v_cvt_pk_bf16_f32 v192, v128, v129\n" \
  "v_cvt_pk_bf16_f32 v193, v130, v131\n" \
  "v_cvt_pk_bf16_f32 v194, v132, v133\n" \
  "v_cvt_pk_bf16_f32 v195, v134, v135\n" \
  "s_waitcnt lgkmcnt(0)\n" \
  "v_mfma_f32_32x32x16_bf16 v[160:175], v[200:203], %[q0a], 0\n" \
  "v_exp_f32 v136, v136\n" \
  "v_exp_f32 v137, v137\n" \
  "v_mfma_f32_32x32x16_bf16 %[Oa], v[206:209], v[192:195], %[Oa]\n" \
  "s_cmp_eq_u32 %[wm3], 0\n" \
  "s_cbranch_scc0 1f\n" \
  "buffer_load_dwordx4 v[48:51], %[vo], %[rw], %[so] offen\n" \
  "buffer_load_dwordx4 v[52:55], %[vo], %[rw], %[so] offen offset:1024\n" \
  "buffer_load_dwordx4 v[56:59], %[vo], %[rw], %[so] offen offset:2048\n" \
  "buffer_load_dwordx4 v[60:63], %[vo], %[rw], %[so] offen offset:3072\n" \
  "s_add_u32 %[so], %[so], 0x1000\n" \
  "1:\n" \
  "v_exp_f32 v138, v138\n" \
  "v_exp_f32 v139, v139\n" \
  "v_exp_f32 v140, v140\n" \
  "v_mfma_f32_32x32x16_bf16 v[176:191], v[200:203], %[q0b], 0\n" \
  "v_exp_f32 v141, v141\n" \
  "v_exp_f32 v142, v142\n" \
  "v_exp_f32 v143, v143\n" \
  "v_cvt_pk_bf16_f32 v196, v136, v137\n" \
  "v_cvt_pk_bf16_f32 v197, v138, v139\n" \
  "v_cvt_pk_bf16_f32 v198, v140, v141\n" \
  "v_cvt_pk_bf16_f32 v199, v142, v143\n" \
  "v_exp_f32 v144, v144\n" \
  "v_exp_f32 v145, v145\n" \
  "v_mfma_f32_32x32x16_bf16 %[Oa], v[210:213], v[196:199], %[Oa]\n" \
  "v_exp_f32 v146, v146\n" \
  "v_exp_f32 v147, v147\n" \
  "v_mfma_f32_32x32x4_2b_bf16 v[160:191], v[204:205], %[q1], v[160:191]\n" \
  "v_exp_f32 v148, v148\n" \
  "v_exp_f32 v149, v149\n" \
  "v_exp_f32 v150, v150\n" \
  "v_exp_f32 v151, v151\n" \
  "v_cvt_pk_bf16_f32 v192, v144, v145\n" \
  "v_cvt_pk_bf16_f32 v193, v146, v147\n" \
  "v_cvt_pk_bf16_f32 v194, v148, v149\n" \
  "v_cvt_pk_bf16_f32 v195, v150, v151\n" \
  "v_exp_f32 v152, v152\n" \
  "v_exp_f32 v153, v153\n" \
  "v_mfma_f32_32x32x16_bf16 %[Ob], v[206:209], v[192:195], %[Ob]\n" \
  "v_exp_f32 v154, v154\n" \
  "v_exp_f32 v155, v155\n" \
  "v_exp_f32 v156, v156\n" \
  "v_exp_f32 v157, v157\n" \
  "v_exp_f32 v158, v158\n" \
  "v_exp_f32 v159, v159\n" \
  "v_cvt_pk_bf16_f32 v196, v152, v153\n" \
  "v_cvt_pk_bf16_f32 v197, v154, v155\n" \
  "v_cvt_pk_bf16_f32 v198, v156, v157\n" \
  "v_cvt_pk_bf16_f32 v199, v158, v159\n" \
  "ds_read_b128 v[200:203], %[pk0] offset:26240\n" \
  "ds_read_b64 v[204:205], %[pk1] offset:27264\n" \
  "v_mfma_f32_32x32x16_bf16 %[Ob], v[210:213], v[196:199], %[Ob]\n" \
  "ds_read_b128 v[206:209], %[pv] offset:24896\n" \
  "ds_read_b128 v[210:213], %[pv] offset:25568\n" \
  "v_exp_f32 v160, v160\n" \
  "v_exp_f32 v161, v161\n" \
  "v_exp_f32 v162, v162\n" \
  "v_exp_f32 v163, v163\n" \
  "v_exp_f32 v164, v164\n" \
  "v_exp_f32 v165, v165\n" \
  "v_exp_f32 v166, v166\n" \
  "v_exp_f32 v167, v167\n" \
  "v_cvt_pk_bf16_f32 v192, v160, v161\n" \
  "v_cvt_pk_bf16_f32 v193, v162, v163\n" \
  "v_cvt_pk_bf16_f32 v194, v164, v165\n" \
  "v_cvt_pk_bf16_f32 v195, v166, v167\n" \
  "s_waitcnt lgkmcnt(0)\n" \
  "v_mfma_f32_32x32x16_bf16 v[128:143], v[200:203], %[q0a], 0\n" \
  "v_exp_f32 v168, v168\n" \
  "v_exp_f32 v169, v169\n" \
  "v_mfma_f32_32x32x16_bf16 %[Oa], v[206:209], v[192:195], %[Oa]\n" \
  "s_cmp_eq_u32 %[wm3], 1\n" \
  "s_cbranch_scc0 1f\n" \
  "buffer_load_dwordx4 v[48:51], %[vo], %[rw], %[so] offen\n" \
  "buffer_load_dwordx4 v[52:55], %[vo], %[rw], %[so] offen offset:1024\n" \
  "buffer_load_dwordx4 v[56:59], %[vo], %[rw], %[so] offen offset:2048\n" \
  "buffer_load_dwordx4 v[60:63], %[vo], %[rw], %[so] offen offset:3072\n" \
  "s_add_u32 %[so], %[so], 0x1000\n" \
  "1:\n" \
  "v_exp_f32 v170, v170\n" \
  "v_exp_f32 v171, v171\n" \
  "v_exp_f32 v172, v172\n" \
  "v_mfma_f32_32x32x16_bf16 v[144:159], v[200:203], %[q0b], 0\n" \
  "v_exp_f32 v173, v173\n" \
  "v_exp_f32 v174, v174\n" \
  "v_exp_f32 v175, v175\n" \
  "v_cvt_pk_bf16_f32 v196, v168, v169\n" \
  "v_cvt_pk_bf16_f32 v197, v170, v171\n" \
  "v_cvt_pk_bf16_f32 v198, v172, v173\n" \
  "v_cvt_pk_bf16_f32 v199, v174, v175\n" \
  "v_exp_f32 v176, v176\n" \
  "v_exp_f32 v177, v177\n" \
  "v_mfma_f32_32x32x16_bf16 %[Oa], v[210:213], v[196:199], %[Oa]\n" \
  "v_exp_f32 v178, v178\n" \
  "v_exp_f32 v179, v179\n" \
  "v_mfma_f32_32x32x4_2b_bf16 v[128:159], v[204:205], %[q1], v[128:159]\n" \
  "v_exp_f32 v180, v180\n" \
  "v_exp_f32 v181, v181\n" \
  "v_exp_f32 v182, v182\n" \
  "v_exp_f32 v183, v183\n" \
  "v_cvt_pk_bf16_f32 v192, v176, v177\n" \
  "v_cvt_pk_bf16_f32 v193, v178, v179\n" \
  "v_cvt_pk_bf16_f32 v194, v180, v181\n" \
  "v_cvt_pk_bf16_f32 v195, v182, v183\n" \
  "v_exp_f32 v184, v184\n" \
  "v_exp_f32 v185, v185\n" \
  "v_mfma_f32_32x32x16_bf16 %[Ob], v[206:209], v[192:195], %[Ob]\n" \
  "v_exp_f32 v186, v186\n" \
  "v_exp_f32 v187, v187\n" \
  "v_exp_f32 v188, v188\n" \
  "v_exp_f32 v189, v189\n" \
  "v_exp_f32 v190, v190\n" \
  "v_exp_f32 v191, v191\n" \
  "v_cvt_pk_bf16_f32 v196, v184, v185\n" \
  "v_cvt_pk_bf16_f32 v197, v186, v187\n" \
  "v_cvt_pk_bf16_f32 v198, v188, v189\n" \
  "v_cvt_pk_bf16_f32 v199, v190, v191\n" \
  "ds_read_b128 v[200:203], %[pk0] offset:28864\n" \
  "ds_read_b64 v[204:205], %[pk1] offset:29888\n" \
  "v_mfma_f32_32x32x16_bf16 %[Ob], v[210:213], v[196:199], %[Ob]\n" \
  "ds_read_b128 v[206:209], %[pv] offset:27520\n" \
  "ds_read_b128 v[210:213], %[pv] offset:28192\n" \
  "v_exp_f32 v128, v128\n" \
  "v_exp_f32 v129, v129\n" \
  "v_exp_f32 v130, v130\n" \
  "v_exp_f32 v131, v131\n" \
  "v_exp_f32 v132, v132\n" \
  "v_exp_f32 v133, v133\n" \
  "v_exp_f32 v134, v134\n" \
  "v_exp_f32 v135, v135\n" \
  "v_cvt_pk_bf16_f32 v192, v128, v129\n" \
  "v_cvt_pk_bf16_f32 v193, v130, v131\n" \
  "v_cvt_pk_bf16_f32 v194, v132, v133\n" \
  "v_cvt_pk_bf16_f32 v195, v134, v135\n" \
  "s_waitcnt lgkmcnt(0)\n" \
  "v_mfma_f32_32x32x16_bf16 v[160:175], v[200:203], %[q0a], 0\n" \
  "v_exp_f32 v136, v136\n" \
  "v_exp_f32 v137, v137\n" \
  "v_mfma_f32_32x32x16_bf16 %[Oa], v[206:209], v[192:195], %[Oa]\n" \
  "s_cmp_eq_u32 %[wm3], 2\n" \
  "s_cbranch_scc0 1f\n" \
  "buffer_load_dwordx4 v[48:51], %[vo], %[rw], %[so] offen\n" \
  "buffer_load_dwordx4 v[52:55], %[vo], %[rw], %[so] offen offset:1024\n" \
  "buffer_load_dwordx4 v[56:59], %[vo], %[rw], %[so] offen offset:2048\n" \
  "buffer_load_dwordx4 v[60:63], %[vo], %[rw], %[so] offen offset:3072\n" \
  "s_add_u32 %[so], %[so], 0x1000\n" \
  "1:\n" \
  "v_exp_f32 v138, v138\n" \
  "v_exp_f32 v139, v139\n" \
  "v_exp_f32 v140, v140\n" \
  "v_mfma_f32_32x32x16_bf16 v[176:191], v[200:203], %[q0b], 0\n" \
  "v_exp_f32 v141, v141\n" \
  "v_exp_f32 v142, v142\n" \
  "v_exp_f32 v143, v143\n" \
  "v_cvt_pk_bf16_f32 v196, v136, v137\n" \
  "v_cvt_pk_bf16_f32 v197, v138, v139\n" \
  "v_cvt_pk_bf16_f32 v198, v140, v141\n" \
  "v_cvt_pk_bf16_f32 v199, v142, v143\n" \
  "v_exp_f32 v144, v144\n" \
  "v_exp_f32 v145, v145\n" \
  "v_mfma_f32_32x32x16_bf16 %[Oa], v[210:213], v[196:199], %[Oa]\n" \
  "v_exp_f32 v146, v146\n" \
  "v_exp_f32 v147, v147\n" \
  "v_mfma_f32_32x32x4_2b_bf16 v[160:191], v[204:205], %[q1], v[160:191]\n" \
  "v_exp_f32 v148, v148\n" \
  "v_exp_f32 v149, v149\n" \
  "v_exp_f32 v150, v150\n" \
  "v_exp_f32 v151, v151\n" \
  "v_cvt_pk_bf16_f32 v192, v144, v145\n" \
  "v_cvt_pk_bf16_f32 v193, v146, v147\n" \
  "v_cvt_pk_bf16_f32 v194, v148, v149\n" \
  "v_cvt_pk_bf16_f32 v195, v150, v151\n" \
  "v_exp_f32 v152, v152\n" \
  "v_exp_f32 v153, v153\n" \
  "v_mfma_f32_32x32x16_bf16 %[Ob], v[206:209], v[192:195], %[Ob]\n" \
  "v_exp_f32 v154, v154\n" \
  "v_exp_f32 v155, v155\n" \
  "v_exp_f32 v156, v156\n" \
  "v_exp_f32 v157, v157\n" \
  "v_exp_f32 v158, v158\n" \
  "v_exp_f32 v159, v159\n" \
  "v_cvt_pk_bf16_f32 v196, v152, v153\n" \
  "v_cvt_pk_bf16_f32 v197, v154, v155\n" \
  "v_cvt_pk_bf16_f32 v198, v156, v157\n" \
  "v_cvt_pk_bf16_f32 v199, v158, v159\n" \
  "ds_read_b128 v[200:203], %[pk0] offset:31488\n" \
  "ds_read_b64 v[204:205], %[pk1] offset:32512\n" \
  "v_mfma_f32_32x32x16_bf16 %[Ob], v[210:213], v[196:199], %[Ob]\n" \
  "ds_read_b128 v[206:209], %[pv] offset:30144\n" \
  "ds_read_b128 v[210:213], %[pv] offset:30816\n" \
  "v_exp_f32 v160, v160\n" \
  "v_exp_f32 v161, v161\n" \
  "v_exp_f32 v162, v162\n" \
  "v_exp_f32 v163, v163\n" \
  "v_exp_f32 v164, v164\n" \
  "v_exp_f32 v165, v165\n" \
  "v_exp_f32 v166, v166\n" \
  "v_exp_f32 v167, v167\n" \
  "v_cvt_pk_bf16_f32 v192, v160, v161\n" \
  "v_cvt_pk_bf16_f32 v193, v162, v163\n" \
  "v_cvt_pk_bf16_f32 v194, v164, v165\n" \
  "v_cvt_pk_bf16_f32 v195, v166, v167\n" \
  "s_waitcnt lgkmcnt(0)\n" \
  "v_mfma_f32_32x32x16_bf16 v[128:143], v[200:203], %[q0a], 0\n" \
  "v_exp_f32 v168, v168\n" \
  "v_exp_f32 v169, v169\n" \
  "v_mfma_f32_32x32x16_bf16 %[Oa], v[206:209], v[192:195], %[Oa]\n" \
  "s_cmp_eq_u32 %[wm3], 0\n" \
  "s_cbranch_scc0 1f\n" \
  "buffer_load_dwordx4 v[64:67], %[vo], %[rw], %[so] offen\n" \
  "buffer_load_dwordx4 v[68:71], %[vo], %[rw], %[so] offen offset:1024\n" \
  "buffer_load_dwordx4 v[72:75], %[vo], %[rw], %[so] offen offset:2048\n" \
  "buffer_load_dwordx4 v[76:79], %[vo], %[rw], %[so] offen offset:3072\n" \
  "s_add_u32 %[so], %[so], 0x1000\n" \
  "1:\n" \
  "v_exp_f32 v170, v170\n" \
  "v_exp_f32 v171, v171\n" \
  "v_exp_f32 v172, v172\n" \
  "v_mfma_f32_32x32x16_bf16 v[144:159], v[200:203], %[q0b], 0\n" \
  "v_exp_f32 v173, v173\n" \
  "v_exp_f32 v174, v174\n" \
  "v_exp_f32 v175, v175\n" \
  "v_cvt_pk_bf16_f32 v196, v168, v169\n" \
  "v_cvt_pk_bf16_f32 v197, v170, v171\n" \
  "v_cvt_pk_bf16_f32 v198, v172, v173\n" \
  "v_cvt_pk_bf16_f32 v199, v174, v175\n" \
  "v_exp_f32 v176, v176\n" \
  "v_exp_f32 v177, v177\n" \
  "v_mfma_f32_32x32x16_bf16 %[Oa], v[210:213], v[196:199], %[Oa]\n" \
  "v_exp_f32 v178, v178\n" \
  "v_exp_f32 v179, v179\n" \
  "v_mfma_f32_32x32x4_2b_bf16 v[128:159], v[204:205], %[q1], v[128:159]\n" \
  "v_exp_f32 v180, v180\n" \
  "v_exp_f32 v181, v181\n" \
  "v_exp_f32 v182, v182\n" \
  "v_exp_f32 v183, v183\n" \
  "v_cvt_pk_bf16_f32 v192, v176, v177\n" \
  "v_cvt_pk_bf16_f32 v193, v178, v179\n" \
  "v_cvt_pk_bf16_f32 v194, v180, v181\n" \
  "v_cvt_pk_bf16_f32 v195, v182, v183\n" \
  "v_exp_f32 v184, v184\n" \
  "v_exp_f32 v185, v185\n" \
  "v_mfma_f32_32x32x16_bf16 %[Ob], v[206:209], v[192:195], %[Ob]\n" \
  "v_exp_f32 v186, v186\n" \
  "v_exp_f32 v187, v187\n" \
  "v_exp_f32 v188, v188\n" \
  "v_exp_f32 v189, v189\n" \
  "v_exp_f32 v190, v190\n" \
  "v_exp_f32 v191, v191\n" \
  "v_cvt_pk_bf16_f32 v196, v184, v185\n" \
  "v_cvt_pk_bf16_f32 v197, v186, v187\n" \
  "v_cvt_pk_bf16_f32 v198, v188, v189\n" \
  "v_cvt_pk_bf16_f32 v199, v190, v191\n" \
  "ds_read_b128 v[200:203], %[pk0] offset:34112\n" \
  "ds_read_b64 v[204:205], %[pk1] offset:35136\n" \
  "v_mfma_f32_32x32x16_bf16 %[Ob], v[210:213], v[196:199], %[Ob]\n" \
  "ds_read_b128 v[206:209], %[pv] offset:32768\n" \
  "ds_read_b128 v[210:213], %[pv] offset:33440\n" \
  "v_exp_f32 v128, v128\n" \
  "v_exp_f32 v129, v129\n" \
  "v_exp_f32 v130, v130\n" \
  "v_exp_f32 v131, v131\n" \
  "v_exp_f32 v132, v132\n" \
  "v_exp_f32 v133, v133\n" \
  "v_exp_f32 v134, v134\n" \
  "v_exp_f32 v135, v135\n" \
  "v_cvt_pk_bf16_f32 v192, v128, v129\n" \
  "v_cvt_pk_bf16_f32 v193, v130, v131\n" \
  "v_cvt_pk_bf16_f32 v194, v132, v133\n" \
  "v_cvt_pk_bf16_f32 v195, v134, v135\n" \
  "s_waitcnt lgkmcnt(0)\n" \
  "v_mfma_f32_32x32x16_bf16 v[160:175], v[200:203], %[q0a], 0\n" \
  "v_exp_f32 v136, v136\n" \
  "v_exp_f32 v137, v137\n" \
  "v_mfma_f32_32x32x16_bf16 %[Oa], v[206:209], v[192:195], %[Oa]\n" \
  "s_cmp_eq_u32 %[wm3], 1\n" \
  "s_cbranch_scc0 1f\n" \
  "buffer_load_dwordx4 v[64:67], %[vo], %[rw], %[so] offen\n" \
  "buffer_load_dwordx4 v[68:71], %[vo], %[rw], %[so] offen offset:1024\n" \
  "buffer_load_dwordx4 v[72:75], %[vo], %[rw], %[so] offen offset:2048\n" \
  "buffer_load_dwordx4 v[76:79], %[vo], %[rw], %[so] offen offset:3072\n" \
  "s_add_u32 %[so], %[so], 0x1000\n" \
  "1:\n" \
  "v_exp_f32 v138, v138\n" \
  "v_exp_f32 v139, v139\n" \
  "v_exp_f32 v140, v140\n" \
  "v_mfma_f32_32x32x16_bf16 v[176:191], v[200:203], %[q0b], 0\n" \
  "v_exp_f32 v141, v141\n" \
  "v_exp_f32 v142, v142\n" \
  "v_exp_f32 v143, v143\n" \
  "v_cvt_pk_bf16_f32 v196, v136, v137\n" \
  "v_cvt_pk_bf16_f32 v197, v138, v139\n" \
  "v_cvt_pk_bf16_f32 v198, v140, v141\n" \
  "v_cvt_pk_bf16_f32 v199, v142, v143\n" \
  "v_exp_f32 v144, v144\n" \
  "v_exp_f32 v145, v145\n" \
  "v_mfma_f32_32x32x16_bf16 %[Oa], v[210:213], v[196:199], %[Oa]\n" \
  "v_exp_f32 v146, v146\n" \
  "v_exp_f32 v147, v147\n" \
  "v_mfma_f32_32x32x4_2b_bf16 v[160:191], v[204:205], %[q1], v[160:191]\n" \
  "v_exp_f32 v148, v148\n" \
  "v_exp_f32 v149, v149\n" \
  "v_exp_f32 v150, v150\n" \
  "v_exp_f32 v151, v151\n" \
  "v_cvt_pk_bf16_f32 v192, v144, v145\n" \
  "v_cvt_pk_bf16_f32 v193, v146, v147\n" \
  "v_cvt_pk_bf16_f32 v194, v148, v149\n" \
  "v_cvt_pk_bf16_f32 v195, v150, v151\n" \
  "v_exp_f32 v152, v152\n" \
  "v_exp_f32 v153, v153\n" \
  "v_mfma_f32_32x32x16_bf16 %[Ob], v[206:209], v[192:195], %[Ob]\n" \
  "v_exp_f32 v154, v154\n" \
  "v_exp_f32 v155, v155\n" \
  "v_exp_f32 v156, v156\n" \
  "v_exp_f32 v157, v157\n" \
  "v_exp_f32 v158, v158\n" \
  "v_exp_f32 v159, v159\n" \
  "v_cvt_pk_bf16_f32 v196, v152, v153\n" \
  "v_cvt_pk_bf16_f32 v197, v154, v155\n" \
  "v_cvt_pk_bf16_f32 v198, v156, v157\n" \
  "v_cvt_pk_bf16_f32 v199, v158, v159\n" \
  "ds_read_b128 v[200:203], %[pk0h] offset:0\n" \
  "ds_read_b64 v[204:205], %[pk1h] offset:1024\n" \
  "v_mfma_f32_32x32x16_bf16 %[Ob], v[210:213], v[196:199], %[Ob]\n" \
  "ds_read_b128 v[206:209], %[pv] offset:35392\n" \
  "ds_read_b128 v[210:213], %[pv] offset:36064\n" \
  "v_exp_f32 v160, v160\n" \
  "v_exp_f32 v161, v161\n" \
  "v_exp_f32 v162, v162\n" \
  "v_exp_f32 v163, v163\n" \
  "v_exp_f32 v164, v164\n" \
  "v_exp_f32 v165, v165\n" \
  "v_exp_f32 v166, v166\n" \
  "v_exp_f32 v167, v167\n" \
  "v_cvt_pk_bf16_f32 v192, v160, v161\n" \
  "v_cvt_pk_bf16_f32 v193, v162, v163\n" \
  "v_cvt_pk_bf16_f32 v194, v164, v165\n" \
  "v_cvt_pk_bf16_f32 v195, v166, v167\n" \
  "s_waitcnt lgkmcnt(0)\n" \
  "v_mfma_f32_32x32x16_bf16 v[128:143], v[200:203], %[q0a], 0\n" \
  "v_exp_f32 v168, v168\n" \
  "v_exp_f32 v169, v169\n" \
  "v_mfma_f32_32x32x16_bf16 %[Oa], v[206:209], v[192:195], %[Oa]\n" \
  "s_cmp_eq_u32 %[wm3], 2\n" \
  "s_cbranch_scc0 1f\n" \
  "buffer_load_dwordx4 v[64:67], %[vo], %[rw], %[so] offen\n" \
  "buffer_load_dwordx4 v[68:71], %[vo], %[rw], %[so] offen offset:1024\n" \
  "buffer_load_dwordx4 v[72:75], %[vo], %[rw], %[so] offen offset:2048\n" \
  "buffer_load_dwordx4 v[76:79], %[vo], %[rw], %[so] offen offset:3072\n" \
  "s_add_u32 %[so], %[so], 0x1000\n" \
  "1:\n" \
  "v_exp_f32 v170, v170\n" \
  "v_exp_f32 v171, v171\n" \
  "v_exp_f32 v172, v172\n" \
  "v_mfma_f32_32x32x16_bf16 v[144:159], v[200:203], %[q0b], 0\n" \
  "v_exp_f32 v173, v173\n" \
  "v_exp_f32 v174, v174\n" \
  "v_exp_f32 v175, v175\n" \
  "v_cvt_pk_bf16_f32 v196, v168, v169\n" \
  "v_cvt_pk_bf16_f32 v197, v170, v171\n" \
  "v_cvt_pk_bf16_f32 v198, v172, v173\n" \
  "v_cvt_pk_bf16_f32 v199, v174, v175\n" \
  "v_exp_f32 v176, v176\n" \
  "v_exp_f32 v177, v177\n" \
  "v_mfma_f32_32x32x16_bf16 %[Oa], v[210:213], v[196:199], %[Oa]\n" \
  "v_exp_f32 v178, v178\n" \
  "v_exp_f32 v179, v179\n" \
  "v_mfma_f32_32x32x4_2b_bf16 v[128:159], v[204:205], %[q1], v[128:159]\n" \
  "v_exp_f32 v180, v180\n" \
  "v_exp_f32 v181, v181\n" \
  "v_exp_f32 v182, v182\n" \
  "v_exp_f32 v183, v183\n" \
  "v_cvt_pk_bf16_f32 v192, v176, v177\n" \
  "v_cvt_pk_bf16_f32 v193, v178, v179\n" \
  "v_cvt_pk_bf16_f32 v194, v180, v181\n" \
  "v_cvt_pk_bf16_f32 v195, v182, v183\n" \
  "v_exp_f32 v184, v184\n" \
  "v_exp_f32 v185, v185\n" \
  "v_mfma_f32_32x32x16_bf16 %[Ob], v[206:209], v[192:195], %[Ob]\n" \
  "v_exp_f32 v186, v186\n" \
  "v_exp_f32 v187, v187\n" \
  "v_exp_f32 v188, v188\n" \
  "v_exp_f32 v189, v189\n" \
  "v_exp_f32 v190, v190\n" \
  "v_exp_f32 v191, v191\n" \
  "v_cvt_pk_bf16_f32 v196, v184, v185\n" \
  "v_cvt_pk_bf16_f32 v197, v186, v187\n" \
  "v_cvt_pk_bf16_f32 v198, v188, v189\n" \
  "v_cvt_pk_bf16_f32 v199, v190, v191\n" \
  "ds_read_b128 v[200:203], %[pk0h] offset:2624\n" \
  "ds_read_b64 v[204:205], %[pk1h] offset:3648\n" \
  "v_mfma_f32_32x32x16_bf16 %[Ob], v[210:213], v[196:199], %[Ob]\n" \
  "ds_read_b128 v[206:209], %[pvh] offset:1280\n" \
  "ds_read_b128 v[210:213], %[pvh] offset:1952\n" \
  "v_exp_f32 v128, v128\n" \
  "v_exp_f32 v129, v129\n" \
  "v_exp_f32 v130, v130\n" \
  "v_exp_f32 v131, v131\n" \
  "v_exp_f32 v132, v132\n" \
  "v_exp_f32 v133, v133\n" \
  "v_exp_f32 v134, v134\n" \
  "v_exp_f32 v135, v135\n" \
  "v_cvt_pk_bf16_f32 v192, v128, v129\n" \
  "v_cvt_pk_bf16_f32 v193, v130, v131\n" \
  "v_cvt_pk_bf16_f32 v194, v132, v133\n" \
  "v_cvt_pk_bf16_f32 v195, v134, v135\n" \
  "s_waitcnt lgkmcnt(0)\n" \
  "v_mfma_f32_32x32x16_bf16 v[160:175], v[200:203], %[q0a], 0\n" \
  "v_exp_f32 v136, v136\n" \
  "v_exp_f32 v137, v137\n" \
  "v_mfma_f32_32x32x16_bf16 %[Oa], v[206:209], v[192:195], %[Oa]\n" \
  "s_cmp_eq_u32 %[wm3], 0\n" \
  "s_cbranch_scc0 1f\n" \
  "buffer_load_dwordx4 v[80:83], %[vo], %[rw], %[so] offen\n" \
  "buffer_load_dwordx4 v[84:87], %[vo], %[rw], %[so] offen offset:1024\n" \
  "buffer_load_dwordx4 v[88:91], %[vo], %[rw], %[so] offen offset:2048\n" \
  "buffer_load_dwordx4 v[92:95], %[vo], %[rw], %[so] offen offset:3072\n" \
  "s_add_u32 %[so], %[so], 0x1000\n" \
  "1:\n" \
  "v_exp_f32 v138, v138\n" \
  "v_exp_f32 v139, v139\n" \
  "v_exp_f32 v140, v140\n" \
  "v_mfma_f32_32x32x16_bf16 v[176:191], v[200:203], %[q0b], 0\n" \
  "v_exp_f32 v141, v141\n" \
  "v_exp_f32 v142, v142\n" \
  "v_exp_f32 v143, v143\n" \
  "v_cvt_pk_bf16_f32 v196, v136, v137\n" \
  "v_cvt_pk_bf16_f32 v197, v138, v139\n" \
  "v_cvt_pk_bf16_f32 v198, v140, v141\n" \
  "v_cvt_pk_bf16_f32 v199, v142, v143\n" \
  "v_exp_f32 v144, v144\n" \
  "v_exp_f32 v145, v145\n" \
  "v_mfma_f32_32x32x16_bf16 %[Oa], v[210:213], v[196:199], %[Oa]\n" \
  "v_exp_f32 v146, v146\n" \
  "v_exp_f32 v147, v147\n" \
  "v_mfma_f32_32x32x4_2b_bf16 v[160:191], v[204:205], %[q1], v[160:191]\n" \
  "v_exp_f32 v148, v148\n" \
  "v_exp_f32 v149, v149\n" \
  "v_exp_f32 v150, v150\n" \
  "v_exp_f32 v151, v151\n" \
  "v_cvt_pk_bf16_f32 v192, v144, v145\n" \
  "v_cvt_pk_bf16_f32 v193, v146, v147\n" \
  "v_cvt_pk_bf16_f32 v194, v148, v149\n" \
  "v_cvt_pk_bf16_f32 v195, v150, v151\n" \
  "v_exp_f32 v152, v152\n" \
  "v_exp_f32 v153, v153\n" \
  "v_mfma_f32_32x32x16_bf16 %[Ob], v[206:209], v[192:195], %[Ob]\n" \
  "v_exp_f32 v154, v154\n" \
  "v_exp_f32 v155, v155\n" \
  "v_exp_f32 v156, v156\n" \
  "v_exp_f32 v157, v157\n" \
  "v_exp_f32 v158, v158\n" \
  "v_exp_f32 v159, v159\n" \
  "v_cvt_pk_bf16_f32 v196, v152, v153\n" \
  "v_cvt_pk_bf16_f32 v197, v154, v155\n" \
  "v_cvt_pk_bf16_f32 v198, v156, v157\n" \
  "v_cvt_pk_bf16_f32 v199, v158, v159\n" \
  "ds_read_b128 v[200:203], %[pk0h] offset:5248\n" \
  "ds_read_b64 v[204:205], %[pk1h] offset:6272\n" \
  "v_mfma_f32_32x32x16_bf16 %[Ob], v[210:213], v[196:199], %[Ob]\n" \
  "ds_read_b128 v[206:209], %[pvh] offset:3904\n" \
  "ds_read_b128 v[210:213], %[pvh] offset:4576\n" \
  "v_exp_f32 v160, v160\n" \
  "v_exp_f32 v161, v161\n" \
  "v_exp_f32 v162, v162\n" \
  "v_exp_f32 v163, v163\n" \
  "v_exp_f32 v164, v164\n" \
  "v_exp_f32 v165, v165\n" \
  "v_exp_f32 v166, v166\n" \
  "v_exp_f32 v167, v167\n" \
  "v_cvt_pk_bf16_f32 v192, v160, v161\n" \
  "v_cvt_pk_bf16_f32 v193, v162, v163\n" \
  "v_cvt_pk_bf16_f32 v194, v164, v165\n" \
  "v_cvt_pk_bf16_f32 v195, v166, v167\n" \
  "s_waitcnt lgkmcnt(0)\n" \
  "v_mfma_f32_32x32x16_bf16 v[128:143], v[200:203], %[q0a], 0\n" \
  "v_exp_f32 v168, v168\n" \
  "v_exp_f32 v169, v169\n" \
  "v_mfma_f32_32x32x16_bf16 %[Oa], v[206:209], v[192:195], %[Oa]\n" \
  "s_cmp_eq_u32 %[wm3], 1\n" \
  "s_cbranch_scc0 1f\n" \
  "buffer_load_dwordx4 v[80:83], %[vo], %[rw], %[so] offen\n" \
  "buffer_load_dwordx4 v[84:87], %[vo], %[rw], %[so] offen offset:1024\n" \
  "buffer_load_dwordx4 v[88:91], %[vo], %[rw], %[so] offen offset:2048\n" \
  "buffer_load_dwordx4 v[92:95], %[vo], %[rw], %[so] offen offset:3072\n" \
  "s_add_u32 %[so], %[so], 0x1000\n" \
  "1:\n" \
  "v_exp_f32 v170, v170\n" \
  "v_exp_f32 v171, v171\n" \
  "v_exp_f32 v172, v172\n" \
  "v_mfma_f32_32x32x16_bf16 v[144:159], v[200:203], %[q0b], 0\n" \
  "v_exp_f32 v173, v173\n" \
  "v_exp_f32 v174, v174\n" \
  "v_exp_f32 v175, v175\n" \
  "v_cvt_pk_bf16_f32 v196, v168, v169\n" \
  "v_cvt_pk_bf16_f32 v197, v170, v171\n" \
  "v_cvt_pk_bf16_f32 v198, v172, v173\n" \
  "v_cvt_pk_bf16_f32 v199, v174, v175\n" \
  "v_exp_f32 v176, v176\n" \
  "v_exp_f32 v177, v177\n" \
  "v_mfma_f32_32x32x16_bf16 %[Oa], v[210:213], v[196:199], %[Oa]\n" \
  "v_exp_f32 v178, v178\n" \
  "v_exp_f32 v179, v179\n" \
  "v_mfma_f32_32x32x4_2b_bf16 v[128:159], v[204:205], %[q1], v[128:159]\n" \
  "v_exp_f32 v180, v180\n" \
  "v_exp_f32 v181, v181\n" \
  "v_exp_f32 v182, v182\n" \
  "v_exp_f32 v183, v183\n" \
  "v_cvt_pk_bf16_f32 v192, v176, v177\n" \
  "v_cvt_pk_bf16_f32 v193, v178, v179\n" \
  "v_cvt_pk_bf16_f32 v194, v180, v181\n" \
  "v_cvt_pk_bf16_f32 v195, v182, v183\n" \
  "v_exp_f32 v184, v184\n" \
  "v_exp_f32 v185, v185\n" \
  "v_mfma_f32_32x32x16_bf16 %[Ob], v[206:209], v[192:195], %[Ob]\n" \
  "v_exp_f32 v186, v186\n" \
  "v_exp_f32 v187, v187\n" \
  "v_exp_f32 v188, v188\n" \
  "v_exp_f32 v189, v189\n" \
  "v_exp_f32 v190, v190\n" \
  "v_exp_f32 v191, v191\n" \
  "v_cvt_pk_bf16_f32 v196, v184, v185\n" \
  "v_cvt_pk_bf16_f32 v197, v186, v187\n" \
  "v_cvt_pk_bf16_f32 v198, v188, v189\n" \
  "v_cvt_pk_bf16_f32 v199, v190, v191\n" \
  "ds_read_b128 v[200:203], %[pk0h] offset:7872\n" \
  "ds_read_b64 v[204:205], %[pk1h] offset:8896\n" \
  "v_mfma_f32_32x32x16_bf16 %[Ob], v[210:213], v[196:199], %[Ob]\n" \
  "ds_read_b128 v[206:209], %[pvh] offset:6528\n" \
  "ds_read_b128 v[210:213], %[pvh] offset:7200\n" \
  "v_exp_f32 v128, v128\n" \
  "v_exp_f32 v129, v129\n" \
  "v_exp_f32 v130, v130\n" \
  "v_exp_f32 v131, v131\n" \
  "v_exp_f32 v132, v132\n" \
  "v_exp_f32 v133, v133\n" \
  "v_exp_f32 v134, v134\n" \
  "v_exp_f32 v135, v135\n" \
  "v_cvt_pk_bf16_f32 v192, v128, v129\n" \
  "v_cvt_pk_bf16_f32 v193, v130, v131\n" \
  "v_cvt_pk_bf16_f32 v194, v132, v133\n" \
  "v_cvt_pk_bf16_f32 v195, v134, v135\n" \
  "s_waitcnt lgkmcnt(0)\n" \
  "v_mfma_f32_32x32x16_bf16 v[160:175], v[200:203], %[q0a], 0\n" \
  "v_exp_f32 v136, v136\n" \
  "v_exp_f32 v137, v137\n" \
  "v_mfma_f32_32x32x16_bf16 %[Oa], v[206:209], v[192:195], %[Oa]\n" \
  "s_cmp_eq_u32 %[wm3], 2\n" \
  "s_cbranch_scc0 1f\n" \
  "buffer_load_dwordx4 v[80:83], %[vo], %[rw], %[so] offen\n" \
  "buffer_load_dwordx4 v[84:87], %[vo], %[rw], %[so] offen offset:1024\n" \
  "buffer_load_dwordx4 v[88:91], %[vo], %[rw], %[so] offen offset:2048\n" \
  "buffer_load_dwordx4 v[92:95], %[vo], %[rw], %[so] offen offset:3072\n" \
  "s_add_u32 %[so], %[so], 0x1000\n" \
  "1:\n" \
  "v_exp_f32 v138, v138\n" \
  "v_exp_f32 v139, v139\n" \
  "v_exp_f32 v140, v140\n" \
  "v_mfma_f32_32x32x16_bf16 v[176:191], v[200:203], %[q0b], 0\n" \
  "v_exp_f32 v141, v141\n" \
  "v_exp_f32 v142, v142\n" \
  "v_exp_f32 v143, v143\n" \
  "v_cvt_pk_bf16_f32 v196, v136, v137\n" \
  "v_cvt_pk_bf16_f32 v197, v138, v139\n" \
  "v_cvt_pk_bf16_f32 v198, v140, v141\n" \
  "v_cvt_pk_bf16_f32 v199, v142, v143\n" \
  "v_exp_f32 v144, v144\n" \
  "v_exp_f32 v145, v145\n" \
  "v_mfma_f32_32x32x16_bf16 %[Oa], v[210:213], v[196:199], %[Oa]\n" \
  "v_exp_f32 v146, v146\n" \
  "v_exp_f32 v147, v147\n" \
  "v_mfma_f32_32x32x4_2b_bf16 v[160:191], v[204:205], %[q1], v[160:191]\n" \
  "v_exp_f32 v148, v148\n" \
  "v_exp_f32 v149, v149\n" \
  "v_exp_f32 v150, v150\n" \
  "v_exp_f32 v151, v151\n" \
  "v_cvt_pk_bf16_f32 v192, v144, v145\n" \
  "v_cvt_pk_bf16_f32 v193, v146, v147\n" \
  "v_cvt_pk_bf16_f32 v194, v148, v149\n" \
  "v_cvt_pk_bf16_f32 v195, v150, v151\n" \
  "v_exp_f32 v152, v152\n" \
  "v_exp_f32 v153, v153\n" \
  "v_mfma_f32_32x32x16_bf16 %[Ob], v[206:209], v[192:195], %[Ob]\n" \
  "v_exp_f32 v154, v154\n" \
  "v_exp_f32 v155, v155\n" \
  "v_exp_f32 v156, v156\n" \
  "v_exp_f32 v157, v157\n" \
  "v_exp_f32 v158, v158\n" \
  "v_exp_f32 v159, v159\n" \
  "v_cvt_pk_bf16_f32 v196, v152, v153\n" \
  "v_cvt_pk_bf16_f32 v197, v154, v155\n" \
  "v_cvt_pk_bf16_f32 v198, v156, v157\n" \
  "v_cvt_pk_bf16_f32 v199, v158, v159\n" \
  "ds_read_b128 v[200:203], %[pk0h] offset:10496\n" \
  "ds_read_b64 v[204:205], %[pk1h] offset:11520\n" \
  "v_mfma_f32_32x32x16_bf16 %[Ob], v[210:213], v[196:199], %[Ob]\n" \
  "ds_read_b128 v[206:209], %[pvh] offset:9152\n" \
  "ds_read_b128 v[210:213], %[pvh] offset:9824\n" \
  "v_exp_f32 v160, v160\n" \
  "v_exp_f32 v161, v161\n" \
  "v_exp_f32 v162, v162\n" \
  "v_exp_f32 v163, v163\n" \
  "v_exp_f32 v164, v164\n" \
  "v_exp_f32 v165, v165\n" \
  "v_exp_f32 v166, v166\n" \
  "v_exp_f32 v167, v167\n" \
  "v_cvt_pk_bf16_f32 v192, v160, v161\n" \
  "v_cvt_pk_bf16_f32 v193, v162, v163\n" \
  "v_cvt_pk_bf16_f32 v194, v164, v165\n" \
  "v_cvt_pk_bf16_f32 v195, v166, v167\n" \
  "s_waitcnt lgkmcnt(0)\n" \
  "v_mfma_f32_32x32x16_bf16 v[128:143], v[200:203], %[q0a], 0\n" \
  "v_exp_f32 v168, v168\n" \
  "v_exp_f32 v169, v169\n" \
  "v_mfma_f32_32x32x16_bf16 %[Oa], v[206:209], v[192:195], %[Oa]\n" \
  "s_cmp_eq_u32 %[wm3], 0\n" \
  "s_cbranch_scc0 1f\n" \
  "buffer_load_dwordx4 v[96:99], %[vo], %[rw], %[so] offen\n" \
  "buffer_load_dwordx4 v[100:103], %[vo], %[rw], %[so] offen offset:1024\n" \
  "buffer_load_dwordx4 v[104:107], %[vo], %[rw], %[so] offen offset:2048\n" \
  "buffer_load_dwordx4 v[108:111], %[vo], %[rw], %[so] offen offset:3072\n" \
  "s_add_u32 %[so], %[so], 0x1000\n" \
  "1:\n" \
  "v_exp_f32 v170, v170\n" \
  "v_exp_f32 v171, v171\n" \
  "v_exp_f32 v172, v172\n" \
  "v_mfma_f32_32x32x16_bf16 v[144:159], v[200:203], %[q0b], 0\n" \
  "v_exp_f32 v173, v173\n" \
  "v_exp_f32 v174, v174\n" \
  "v_exp_f32 v175, v175\n" \
  "v_cvt_pk_bf16_f32 v196, v168, v169\n" \
  "v_cvt_pk_bf16_f32 v197, v170, v171\n" \
  "v_cvt_pk_bf16_f32 v198, v172, v173\n" \
  "v_cvt_pk_bf16_f32 v199, v174, v175\n" \
  "v_exp_f32 v176, v176\n" \
  "v_exp_f32 v177, v177\n" \
  "v_mfma_f32_32x32x16_bf16 %[Oa], v[210:213], v[196:199], %[Oa]\n" \
  "v_exp_f32 v178, v178\n" \
  "v_exp_f32 v179, v179\n" \
  "v_mfma_f32_32x32x4_2b_bf16 v[128:159], v[204:205], %[q1], v[128:159]\n" \
  "v_exp_f32 v180, v180\n" \
  "v_exp_f32 v181, v181\n" \
  "v_exp_f32 v182, v182\n" \
  "v_exp_f32 v183, v183\n" \
  "v_cvt_pk_bf16_f32 v192, v176, v177\n" \
  "v_cvt_pk_bf16_f32 v193, v178, v179\n" \
  "v_cvt_pk_bf16_f32 v194, v180, v181\n" \
  "v_cvt_pk_bf16_f32 v195, v182, v183\n" \
  "v_exp_f32 v184, v184\n" \
  "v_exp_f32 v185, v185\n" \
  "v_mfma_f32_32x32x16_bf16 %[Ob], v[206:209], v[192:195], %[Ob]\n" \
  "v_exp_f32 v186, v186\n" \
  "v_exp_f32 v187, v187\n" \
  "v_exp_f32 v188, v188\n" \
  "v_exp_f32 v189, v189\n" \
  "v_exp_f32 v190, v190\n" \
  "v_exp_f32 v191, v191\n" \
  "v_cvt_pk_bf16_f32 v196, v184, v185\n" \
  "v_cvt_pk_bf16_f32 v197, v186, v187\n" \
  "v_cvt_pk_bf16_f32 v198, v188, v189\n" \
  "v_cvt_pk_bf16_f32 v199, v190, v191\n" \
  "ds_read_b128 v[200:203], %[pk0h] offset:13120\n" \
  "ds_read_b64 v[204:205], %[pk1h] offset:14144\n" \
  "v_mfma_f32_32x32x16_bf16 %[Ob], v[210:213], v[196:199], %[Ob]\n" \
  "ds_read_b128 v[206:209], %[pvh] offset:11776\n" \
  "ds_read_b128 v[210:213], %[pvh] offset:12448\n" \
  "v_exp_f32 v128, v128\n" \
  "v_exp_f32 v129, v129\n" \
  "v_exp_f32 v130, v130\n" \
  "v_exp_f32 v131, v131\n" \
  "v_exp_f32 v132, v132\n" \
  "v_exp_f32 v133, v133\n" \
  "v_exp_f32 v134, v134\n" \
  "v_exp_f32 v135, v135\n" \
  "v_cvt_pk_bf16_f32 v192, v128, v129\n" \
  "v_cvt_pk_bf16_f32 v193, v130, v131\n" \
  "v_cvt_pk_bf16_f32 v194, v132, v133\n" \
  "v_cvt_pk_bf16_f32 v195, v134, v135\n" \
  "s_waitcnt lgkmcnt(0)\n" \
  "v_mfma_f32_32x32x16_bf16 v[160:175], v[200:203], %[q0a], 0\n" \
  "v_exp_f32 v136, v136\n" \
  "v_exp_f32 v137, v137\n" \
  "v_mfma_f32_32x32x16_bf16 %[Oa], v[206:209], v[192:195], %[Oa]\n" \
  "s_cmp_eq_u32 %[wm3], 1\n" \
  "s_cbranch_scc0 1f\n" \
  "buffer_load_dwordx4 v[96:99], %[vo], %[rw], %[so] offen\n" \
  "buffer_load_dwordx4 v[100:103], %[vo], %[rw], %[so] offen offset:1024\n" \
  "buffer_load_dwordx4 v[104:107], %[vo], %[rw], %[so] offen offset:2048\n" \
  "buffer_load_dwordx4 v[108:111], %[vo], %[rw], %[so] offen offset:3072\n" \
  "s_add_u32 %[so], %[so], 0x1000\n" \
  "1:\n" \
  "v_exp_f32 v138, v138\n" \
  "v_exp_f32 v139, v139\n" \
  "v_exp_f32 v140, v140\n" \
  "v_mfma_f32_32x32x16_bf16 v[176:191], v[200:203], %[q0b], 0\n" \
  "v_exp_f32 v141, v141\n" \
  "v_exp_f32 v142, v142\n" \
  "v_exp_f32 v143, v143\n" \
  "v_cvt_pk_bf16_f32 v196, v136, v137\n" \
  "v_cvt_pk_bf16_f32 v197, v138, v139\n" \
  "v_cvt_pk_bf16_f32 v198, v140, v141\n" \
  "v_cvt_pk_bf16_f32 v199, v142, v143\n" \
  "v_exp_f32 v144, v144\n" \
  "v_exp_f32 v145, v145\n" \
  "v_mfma_f32_32x32x16_bf16 %[Oa], v[210:213], v[196:199], %[Oa]\n" \
  "v_exp_f32 v146, v146\n" \
  "v_exp_f32 v147, v147\n" \
  "v_mfma_f32_32x32x4_2b_bf16 v[160:191], v[204:205], %[q1], v[160:191]\n" \
  "v_exp_f32 v148, v148\n" \
  "v_exp_f32 v149, v149\n" \
  "v_exp_f32 v150, v150\n" \
  "v_exp_f32 v151, v151\n" \
  "v_cvt_pk_bf16_f32 v192, v144, v145\n" \
  "v_cvt_pk_bf16_f32 v193, v146, v147\n" \
  "v_cvt_pk_bf16_f32 v194, v148, v149\n" \
  "v_cvt_pk_bf16_f32 v195, v150, v151\n" \
  "v_exp_f32 v152, v152\n" \
  "v_exp_f32 v153, v153\n" \
  "v_mfma_f32_32x32x16_bf16 %[Ob], v[206:209], v[192:195], %[Ob]\n" \
  "v_exp_f32 v154, v154\n" \
  "v_exp_f32 v155, v155\n" \
  "v_exp_f32 v156, v156\n" \
  "v_exp_f32 v157, v157\n" \
  "v_exp_f32 v158, v158\n" \
  "v_exp_f32 v159, v159\n" \
  "v_cvt_pk_bf16_f32 v196, v152, v153\n" \
  "v_cvt_pk_bf16_f32 v197, v154, v155\n" \
  "v_cvt_pk_bf16_f32 v198, v156, v157\n" \
  "v_cvt_pk_bf16_f32 v199, v158, v159\n" \
  "ds_read_b128 v[200:203], %[pk0h] offset:15744\n" \
  "ds_read_b64 v[204:205], %[pk1h] offset:16768\n" \
  "v_mfma_f32_32x32x16_bf16 %[Ob], v[210:213], v[196:199], %[Ob]\n" \
  "ds_read_b128 v[206:209], %[pvh] offset:14400\n" \
  "ds_read_b128 v[210:213], %[pvh] offset:15072\n" \
  "v_exp_f32 v160, v160\n" \
  "v_exp_f32 v161, v161\n" \
  "v_exp_f32 v162, v162\n" \
  "v_exp_f32 v163, v163\n" \
  "v_exp_f32 v164, v164\n" \
  "v_exp_f32 v165, v165\n" \
  "v_exp_f32 v166, v166\n" \
  "v_exp_f32 v167, v167\n" \
  "v_cvt_pk_bf16_f32 v192, v160, v161\n" \
  "v_cvt_pk_bf16_f32 v193, v162, v163\n" \
  "v_cvt_pk_bf16_f32 v194, v164, v165\n" \
  "v_cvt_pk_bf16_f32 v195, v166, v167\n" \
  "s_waitcnt lgkmcnt(0)\n" \
  "v_mfma_f32_32x32x16_bf16 v[128:143], v[200:203], %[q0a], 0\n" \
  "v_exp_f32 v168, v168\n" \
  "v_exp_f32 v169, v169\n" \
  "v_mfma_f32_32x32x16_bf16 %[Oa], v[206:209], v[192:195], %[Oa]\n" \
  "s_cmp_eq_u32 %[wm3], 2\n" \
  "s_cbranch_scc0 1f\n" \
  "buffer_load_dwordx4 v[96:99], %[vo], %[rw], %[so] offen\n" \
  "buffer_load_dwordx4 v[100:103], %[vo], %[rw], %[so] offen offset:1024\n" \
  "buffer_load_dwordx4 v[104:107], %[vo], %[rw], %[so] offen offset:2048\n" \
  "buffer_load_dwordx4 v[108:111], %[vo], %[rw], %[so] offen offset:3072\n" \
  "s_add_u32 %[so], %[so], 0x1000\n" \
  "1:\n" \
  "v_exp_f32 v170, v170\n" \
  "v_exp_f32 v171, v171\n" \
  "v_exp_f32 v172, v172\n" \
  "v_mfma_f32_32x32x16_bf16 v[144:159], v[200:203], %[q0b], 0\n" \
  "v_exp_f32 v173, v173\n" \
  "v_exp_f32 v174, v174\n" \
  "v_exp_f32 v175, v175\n" \
  "v_cvt_pk_bf16_f32 v196, v168, v169\n" \
  "v_cvt_pk_bf16_f32 v197, v170, v171\n" \
  "v_cvt_pk_bf16_f32 v198, v172, v173\n" \
  "v_cvt_pk_bf16_f32 v199, v174, v175\n" \
  "v_exp_f32 v176, v176\n" \
  "v_exp_f32 v177, v177\n" \
  "v_mfma_f32_32x32x16_bf16 %[Oa], v[210:213], v[196:199], %[Oa]\n" \
  "v_exp_f32 v178, v178\n" \
  "v_exp_f32 v179, v179\n" \
  "v_mfma_f32_32x32x4_2b_bf16 v[128:159], v[204:205], %[q1], v[128:159]\n" \
  "v_exp_f32 v180, v180\n" \
  "v_exp_f32 v181, v181\n" \
  "v_exp_f32 v182, v182\n" \
  "v_exp_f32 v183, v183\n" \
  "v_cvt_pk_bf16_f32 v192, v176, v177\n" \
  "v_cvt_pk_bf16_f32 v193, v178, v179\n" \
  "v_cvt_pk_bf16_f32 v194, v180, v181\n" \
  "v_cvt_pk_bf16_f32 v195, v182, v183\n" \
  "v_exp_f32 v184, v184\n" \
  "v_exp_f32 v185, v185\n" \
  "v_mfma_f32_32x32x16_bf16 %[Ob], v[206:209], v[192:195], %[Ob]\n" \
  "v_exp_f32 v186, v186\n" \
  "v_exp_f32 v187, v187\n" \
  "v_exp_f32 v188, v188\n" \
  "v_exp_f32 v189, v189\n" \
  "v_exp_f32 v190, v190\n" \
  "v_exp_f32 v191, v191\n" \
  "v_cvt_pk_bf16_f32 v196, v184, v185\n" \
  "v_cvt_pk_bf16_f32 v197, v186, v187\n" \
  "v_cvt_pk_bf16_f32 v198, v188, v189\n" \
  "v_cvt_pk_bf16_f32 v199, v190, v191\n" \
  "ds_read_b128 v[200:203], %[pk0h] offset:18368\n" \
  "ds_read_b64 v[204:205], %[pk1h] offset:19392\n" \
  "v_mfma_f32_32x32x16_bf16 %[Ob], v[210:213], v[196:199], %[Ob]\n" \
  "ds_read_b128 v[206:209], %[pvh] offset:17024\n" \
  "ds_read_b128 v[210:213], %[pvh] offset:17696\n" \
  "v_exp_f32 v128, v128\n" \
  "v_exp_f32 v129, v129\n" \
  "v_exp_f32 v130, v130\n" \
  "v_exp_f32 v131, v131\n" \
  "v_exp_f32 v132, v132\n" \
  "v_exp_f32 v133, v133\n" \
  "v_exp_f32 v134, v134\n" \
  "v_exp_f32 v135, v135\n" \
  "v_cvt_pk_bf16_f32 v192, v128, v129\n" \
  "v_cvt_pk_bf16_f32 v193, v130, v131\n" \
  "v_cvt_pk_bf16_f32 v194, v132, v133\n" \
  "v_cvt_pk_bf16_f32 v195, v134, v135\n" \
  "s_waitcnt lgkmcnt(0)\n" \
  "v_mfma_f32_32x32x16_bf16 v[160:175], v[200:203], %[q0a], 0\n" \
  "v_exp_f32 v136, v136\n" \
  "v_exp_f32 v137, v137\n" \
  "v_mfma_f32_32x32x16_bf16 %[Oa], v[206:209], v[192:195], %[Oa]\n" \
  "s_cmp_eq_u32 %[wm3], 0\n" \
  "s_cbranch_scc0 1f\n" \
  "buffer_load_dwordx4 v[112:115], %[vo], %[rw], %[so] offen\n" \
  "buffer_load_dwordx4 v[116:119], %[vo], %[rw], %[so] offen offset:1024\n" \
  "buffer_load_dwordx4 v[120:123], %[vo], %[rw], %[so] offen offset:2048\n" \
  "buffer_load_dwordx4 v[124:127], %[vo], %[rw], %[so] offen offset:3072\n" \
  "s_add_u32 %[so], %[so], 0x1000\n" \
  "1:\n" \
  "v_exp_f32 v138, v138\n" \
  "v_exp_f32 v139, v139\n" \
  "v_exp_f32 v140, v140\n" \
  "v_mfma_f32_32x32x16_bf16 v[176:191], v[200:203], %[q0b], 0\n" \
  "v_exp_f32 v141, v141\n" \
  "v_exp_f32 v142, v142\n" \
  "v_exp_f32 v143, v143\n" \
  "v_cvt_pk_bf16_f32 v196, v136, v137\n" \
  "v_cvt_pk_bf16_f32 v197, v138, v139\n" \
  "v_cvt_pk_bf16_f32 v198, v140, v141\n" \
  "v_cvt_pk_bf16_f32 v199, v142, v143\n" \
  "v_exp_f32 v144, v144\n" \
  "v_exp_f32 v145, v145\n" \
  "v_mfma_f32_32x32x16_bf16 %[Oa], v[210:213], v[196:199], %[Oa]\n" \
  "v_exp_f32 v146, v146\n" \
  "v_exp_f32 v147, v147\n" \
  "v_mfma_f32_32x32x4_2b_bf16 v[160:191], v[204:205], %[q1], v[160:191]\n" \
  "v_exp_f32 v148, v148\n" \
  "v_exp_f32 v149, v149\n" \
  "v_exp_f32 v150, v150\n" \
  "v_exp_f32 v151, v151\n" \
  "v_cvt_pk_bf16_f32 v192, v144, v145\n" \
  "v_cvt_pk_bf16_f32 v193, v146, v147\n" \
  "v_cvt_pk_bf16_f32 v194, v148, v149\n" \
  "v_cvt_pk_bf16_f32 v195, v150, v151\n" \
  "v_exp_f32 v152, v152\n" \
  "v_exp_f32 v153, v153\n" \
  "v_mfma_f32_32x32x16_bf16 %[Ob], v[206:209], v[192:195], %[Ob]\n" \
  "v_exp_f32 v154, v154\n" \
  "v_exp_f32 v155, v155\n" \
  "v_exp_f32 v156, v156\n" \
  "v_exp_f32 v157, v157\n" \
  "v_exp_f32 v158, v158\n" \
  "v_exp_f32 v159, v159\n" \
  "v_cvt_pk_bf16_f32 v196, v152, v153\n" \
  "v_cvt_pk_bf16_f32 v197, v154, v155\n" \
  "v_cvt_pk_bf16_f32 v198, v156, v157\n" \
  "v_cvt_pk_bf16_f32 v199, v158, v159\n" \
  "ds_read_b128 v[200:203], %[pk0h] offset:20992\n" \
  "ds_read_b64 v[204:205], %[pk1h] offset:22016\n" \
  "v_mfma_f32_32x32x16_bf16 %[Ob], v[210:213], v[196:199], %[Ob]\n" \
  "ds_read_b128 v[206:209], %[pvh] offset:19648\n" \
  "ds_read_b128 v[210:213], %[pvh] offset:20320\n" \
  "v_exp_f32 v160, v160\n" \
  "v_exp_f32 v161, v161\n" \
  "v_exp_f32 v162, v162\n" \
  "v_exp_f32 v163, v163\n" \
  "v_exp_f32 v164, v164\n" \
  "v_exp_f32 v165, v165\n" \
  "v_exp_f32 v166, v166\n" \
  "v_exp_f32 v167, v167\n" \
  "v_cvt_pk_bf16_f32 v192, v160, v161\n" \
  "v_cvt_pk_bf16_f32 v193, v162, v163\n" \
  "v_cvt_pk_bf16_f32 v194, v164, v165\n" \
  "v_cvt_pk_bf16_f32 v195, v166, v167\n" \
  "s_waitcnt lgkmcnt(0)\n" \
  "v_mfma_f32_32x32x16_bf16 v[128:143], v[200:203], %[q0a], 0\n" \
  "v_exp_f32 v168, v168\n" \
  "v_exp_f32 v169, v169\n" \
  "v_mfma_f32_32x32x16_bf16 %[Oa], v[206:209], v[192:195], %[Oa]\n" \
  "s_cmp_eq_u32 %[wm3], 1\n" \
  "s_cbranch_scc0 1f\n" \
  "buffer_load_dwordx4 v[112:115], %[vo], %[rw], %[so] offen\n" \
  "buffer_load_dwordx4 v[116:119], %[vo], %[rw], %[so] offen offset:1024\n" \
  "buffer_load_dwordx4 v[120:123], %[vo], %[rw], %[so] offen offset:2048\n" \
  "buffer_load_dwordx4 v[124:127], %[vo], %[rw], %[so] offen offset:3072\n" \
  "s_add_u32 %[so], %[so], 0x1000\n" \
  "1:\n" \
  "v_exp_f32 v170, v170\n" \
  "v_exp_f32 v171, v171\n" \
  "v_exp_f32 v172, v172\n" \
  "v_mfma_f32_32x32x16_bf16 v[144:159], v[200:203], %[q0b], 0\n" \
  "v_exp_f32 v173, v173\n" \
  "v_exp_f32 v174, v174\n" \
  "v_exp_f32 v175, v175\n" \
  "v_cvt_pk_bf16_f32 v196, v168, v169\n" \
  "v_cvt_pk_bf16_f32 v197, v170, v171\n" \
  "v_cvt_pk_bf16_f32 v198, v172, v173\n" \
  "v_cvt_pk_bf16_f32 v199, v174, v175\n" \
  "v_exp_f32 v176, v176\n" \
  "v_exp_f32 v177, v177\n" \
  "v_mfma_f32_32x32x16_bf16 %[Oa], v[210:213], v[196:199], %[Oa]\n" \
  "v_exp_f32 v178, v178\n" \
  "v_exp_f32 v179, v179\n" \
  "v_mfma_f32_32x32x4_2b_bf16 v[128:159], v[204:205], %[q1], v[128:159]\n" \
  "v_exp_f32 v180, v180\n" \
  "v_exp_f32 v181, v181\n" \
  "v_exp_f32 v182, v182\n" \
  "v_exp_f32 v183, v183\n" \
  "v_cvt_pk_bf16_f32 v192, v176, v177\n" \
  "v_cvt_pk_bf16_f32 v193, v178, v179\n" \
  "v_cvt_pk_bf16_f32 v194, v180, v181\n" \
  "v_cvt_pk_bf16_f32 v195, v182, v183\n" \
  "v_exp_f32 v184, v184\n" \
  "v_exp_f32 v185, v185\n" \
  "v_mfma_f32_32x32x16_bf16 %[Ob], v[206:209], v[192:195], %[Ob]\n" \
  "v_exp_f32 v186, v186\n" \
  "v_exp_f32 v187, v187\n" \
  "v_exp_f32 v188, v188\n" \
  "v_exp_f32 v189, v189\n" \
  "v_exp_f32 v190, v190\n" \
  "v_exp_f32 v191, v191\n" \
  "v_cvt_pk_bf16_f32 v196, v184, v185\n" \
  "v_cvt_pk_bf16_f32 v197, v186, v187\n" \
  "v_cvt_pk_bf16_f32 v198, v188, v189\n" \
  "v_cvt_pk_bf16_f32 v199, v190, v191\n" \
  "ds_read_b128 v[200:203], %[pk0h] offset:23616\n" \
  "ds_read_b64 v[204:205], %[pk1h] offset:24640\n" \
  "v_mfma_f32_32x32x16_bf16 %[Ob], v[210:213], v[196:199], %[Ob]\n" \
  "ds_read_b128 v[206:209], %[pvh] offset:22272\n" \
  "ds_read_b128 v[210:213], %[pvh] offset:22944\n" \
  "v_exp_f32 v128, v128\n" \
  "v_exp_f32 v129, v129\n" \
  "v_exp_f32 v130, v130\n" \
  "v_exp_f32 v131, v131\n" \
  "v_exp_f32 v132, v132\n" \
  "v_exp_f32 v133, v133\n" \
  "v_exp_f32 v134, v134\n" \
  "v_exp_f32 v135, v135\n" \
  "v_cvt_pk_bf16_f32 v192, v128, v129\n" \
  "v_cvt_pk_bf16_f32 v193, v130, v131\n" \
  "v_cvt_pk_bf16_f32 v194, v132, v133\n" \
  "v_cvt_pk_bf16_f32 v195, v134, v135\n" \
  "s_waitcnt lgkmcnt(0)\n" \
  "v_mfma_f32_32x32x16_bf16 v[160:175], v[200:203], %[q0a], 0\n" \
  "v_exp_f32 v136, v136\n" \
  "v_exp_f32 v137, v137\n" \
  "v_mfma_f32_32x32x16_bf16 %[Oa], v[206:209], v[192:195], %[Oa]\n" \
  "s_cmp_eq_u32 %[wm3], 2\n" \
  "s_cbranch_scc0 1f\n" \
  "buffer_load_dwordx4 v[112:115], %[vo], %[rw], %[so] offen\n" \
  "buffer_load_dwordx4 v[116:119], %[vo], %[rw], %[so] offen offset:1024\n" \
  "buffer_load_dwordx4 v[120:123], %[vo], %[rw], %[so] offen offset:2048\n" \
  "buffer_load_dwordx4 v[124:127], %[vo], %[rw], %[so] offen offset:3072\n" \
  "s_add_u32 %[so], %[so], 0x1000\n" \
  "1:\n" \
  "v_exp_f32 v138, v138\n" \
  "v_exp_f32 v139, v139\n" \
  "v_exp_f32 v140, v140\n" \
  "v_mfma_f32_32x32x16_bf16 v[176:191], v[200:203], %[q0b], 0\n" \
  "v_exp_f32 v141, v141\n" \
  "v_exp_f32 v142, v142\n" \
  "v_exp_f32 v143, v143\n" \
  "v_cvt_pk_bf16_f32 v196, v136, v137\n" \
  "v_cvt_pk_bf16_f32 v197, v138, v139\n" \
  "v_cvt_pk_bf16_f32 v198, v140, v141\n" \
  "v_cvt_pk_bf16_f32 v199, v142, v143\n" \
  "v_exp_f32 v144, v144\n" \
  "v_exp_f32 v145, v145\n" \
  "v_mfma_f32_32x32x16_bf16 %[Oa], v[210:213], v[196:199], %[Oa]\n" \
  "v_exp_f32 v146, v146\n" \
  "v_exp_f32 v147, v147\n" \
  "v_mfma_f32_32x32x4_2b_bf16 v[160:191], v[204:205], %[q1], v[160:191]\n" \
  "v_exp_f32 v148, v148\n" \
  "v_exp_f32 v149, v149\n" \
  "v_exp_f32 v150, v150\n" \
  "v_exp_f32 v151, v151\n" \
  "v_cvt_pk_bf16_f32 v192, v144, v145\n" \
  "v_cvt_pk_bf16_f32 v193, v146, v147\n" \
  "v_cvt_pk_bf16_f32 v194, v148, v149\n" \
  "v_cvt_pk_bf16_f32 v195, v150, v151\n" \
  "v_exp_f32 v152, v152\n" \
  "v_exp_f32 v153, v153\n" \
  "v_mfma_f32_32x32x16_bf16 %[Ob], v[206:209], v[192:195], %[Ob]\n" \
  "v_exp_f32 v154, v154\n" \
  "v_exp_f32 v155, v155\n" \
  "v_exp_f32 v156, v156\n" \
  "v_exp_f32 v157, v157\n" \
  "v_exp_f32 v158, v158\n" \
  "v_exp_f32 v159, v159\n" \
  "v_cvt_pk_bf16_f32 v196, v152, v153\n" \
  "v_cvt_pk_bf16_f32 v197, v154, v155\n" \
  "v_cvt_pk_bf16_f32 v198, v156, v157\n" \
  "v_cvt_pk_bf16_f32 v199, v158, v159\n" \
  "ds_read_b128 v[200:203], %[pk0h] offset:26240\n" \
  "ds_read_b64 v[204:205], %[pk1h] offset:27264\n" \
  "v_mfma_f32_32x32x16_bf16 %[Ob], v[210:213], v[196:199], %[Ob]\n" \
  "ds_read_b128 v[206:209], %[pvh] offset:24896\n" \
  "ds_read_b128 v[210:213], %[pvh] offset:25568\n" \
  "v_exp_f32 v160, v160\n" \
  "v_exp_f32 v161, v161\n" \
  "v_exp_f32 v162, v162\n" \
  "v_exp_f32 v163, v163\n" \
  "v_exp_f32 v164, v164\n" \
  "v_exp_f32 v165, v165\n" \
  "v_exp_f32 v166, v166\n" \
  "v_exp_f32 v167, v167\n" \
  "v_cvt_pk_bf16_f32 v192, v160, v161\n" \
  "v_cvt_pk_bf16_f32 v193, v162, v163\n" \
  "v_cvt_pk_bf16_f32 v194, v164, v165\n" \
  "v_cvt_pk_bf16_f32 v195, v166, v167\n" \
  "s_waitcnt lgkmcnt(0)\n" \
  "v_mfma_f32_32x32x16_bf16 v[128:143], v[200:203], %[q0a], 0\n" \
  "v_exp_f32 v168, v168\n" \
  "v_exp_f32 v169, v169\n" \
  "v_mfma_f32_32x32x16_bf16 %[Oa], v[206:209], v[192:195], %[Oa]\n" \
  "v_exp_f32 v170, v170\n" \
  "v_exp_f32 v171, v171\n" \
  "v_exp_f32 v172, v172\n" \
  "v_mfma_f32_32x32x16_bf16 v[144:159], v[200:203], %[q0b], 0\n" \
  "v_exp_f32 v173, v173\n" \
  "v_exp_f32 v174, v174\n" \
  "v_exp_f32 v175, v175\n" \
  "v_cvt_pk_bf16_f32 v196, v168, v169\n" \
  "v_cvt_pk_bf16_f32 v197, v170, v171\n" \
  "v_cvt_pk_bf16_f32 v198, v172, v173\n" \
  "v_cvt_pk_bf16_f32 v199, v174, v175\n" \
  "v_exp_f32 v176, v176\n" \
  "v_exp_f32 v177, v177\n" \
  "v_mfma_f32_32x32x16_bf16 %[Oa], v[210:213], v[196:199], %[Oa]\n" \
  "v_exp_f32 v178, v178\n" \
  "v_exp_f32 v179, v179\n" \
  "v_mfma_f32_32x32x4_2b_bf16 v[128:159], v[204:205], %[q1], v[128:159]\n" \
  "v_exp_f32 v180, v180\n" \
  "v_exp_f32 v181, v181\n" \
  "v_exp_f32 v182, v182\n" \
  "v_exp_f32 v183, v183\n" \
  "v_cvt_pk_bf16_f32 v192, v176, v177\n" \
  "v_cvt_pk_bf16_f32 v193, v178, v179\n" \
  "v_cvt_pk_bf16_f32 v194, v180, v181\n" \
  "v_cvt_pk_bf16_f32 v195, v182, v183\n" \
  "v_exp_f32 v184, v184\n" \
  "v_exp_f32 v185, v185\n" \
  "v_mfma_f32_32x32x16_bf16 %[Ob], v[206:209], v[192:195], %[Ob]\n" \
  "v_exp_f32 v186, v186\n" \
  "v_exp_f32 v187, v187\n" \
  "v_exp_f32 v188, v188\n" \
  "v_exp_f32 v189, v189\n" \
  "v_exp_f32 v190, v190\n" \
  "v_exp_f32 v191, v191\n" \
  "v_cvt_pk_bf16_f32 v196, v184, v185\n" \
  "v_cvt_pk_bf16_f32 v197, v186, v187\n" \
  "v_cvt_pk_bf16_f32 v198, v188, v189\n" \
  "v_cvt_pk_bf16_f32 v199, v190, v191\n" \
  "ds_read_b128 v[200:203], %[pk0h] offset:28864\n" \
  "ds_read_b64 v[204:205], %[pk1h] offset:29888\n" \
  "v_mfma_f32_32x32x16_bf16 %[Ob], v[210:213], v[196:199], %[Ob]\n" \
  "ds_read_b128 v[206:209], %[pvh] offset:27520\n" \
  "ds_read_b128 v[210:213], %[pvh] offset:28192\n" \
  "v_exp_f32 v128, v128\n" \
  "v_exp_f32 v129, v129\n" \
  "v_exp_f32 v130, v130\n" \
  "v_exp_f32 v131, v131\n" \
  "v_exp_f32 v132, v132\n" \
  "v_exp_f32 v133, v133\n" \
  "v_exp_f32 v134, v134\n" \
  "v_exp_f32 v135, v135\n" \
  "v_cvt_pk_bf16_f32 v192, v128, v129\n" \
  "v_cvt_pk_bf16_f32 v193, v130, v131\n" \
  "v_cvt_pk_bf16_f32 v194, v132, v133\n" \
  "v_cvt_pk_bf16_f32 v195, v134, v135\n" \
  "s_waitcnt lgkmcnt(0)\n" \
  "v_mfma_f32_32x32x16_bf16 v[160:175], v[200:203], %[q0a], 0\n" \
  "v_exp_f32 v136, v136\n" \
  "v_exp_f32 v137, v137\n" \
  "v_mfma_f32_32x32x16_bf16 %[Oa], v[206:209], v[192:195], %[Oa]\n" \
  "v_exp_f32 v138, v138\n" \
  "v_exp_f32 v139, v139\n" \
  "v_exp_f32 v140, v140\n" \
  "v_mfma_f32_32x32x16_bf16 v[176:191], v[200:203], %[q0b], 0\n" \
  "v_exp_f32 v141, v141\n" \
  "v_exp_f32 v142, v142\n" \
  "v_exp_f32 v143, v143\n" \
  "v_cvt_pk_bf16_f32 v196, v136, v137\n" \
  "v_cvt_pk_bf16_f32 v197, v138, v139\n" \
  "v_cvt_pk_bf16_f32 v198, v140, v141\n" \
  "v_cvt_pk_bf16_f32 v199, v142, v143\n" \
  "v_exp_f32 v144, v144\n" \
  "v_exp_f32 v145, v145\n" \
  "v_mfma_f32_32x32x16_bf16 %[Oa], v[210:213], v[196:199], %[Oa]\n" \
  "v_exp_f32 v146, v146\n" \
  "v_exp_f32 v147, v147\n" \
  "v_mfma_f32_32x32x4_2b_bf16 v[160:191], v[204:205], %[q1], v[160:191]\n" \
  "v_exp_f32 v148, v148\n" \
  "v_exp_f32 v149, v149\n" \
  "v_exp_f32 v150, v150\n" \
  "v_exp_f32 v151, v151\n" \
  "v_cvt_pk_bf16_f32 v192, v144, v145\n" \
  "v_cvt_pk_bf16_f32 v193, v146, v147\n" \
  "v_cvt_pk_bf16_f32 v194, v148, v149\n" \
  "v_cvt_pk_bf16_f32 v195, v150, v151\n" \
  "v_exp_f32 v152, v152\n" \
  "v_exp_f32 v153, v153\n" \
  "v_mfma_f32_32x32x16_bf16 %[Ob], v[206:209], v[192:195], %[Ob]\n" \
  "v_exp_f32 v154, v154\n" \
  "v_exp_f32 v155, v155\n" \
  "v_exp_f32 v156, v156\n" \
  "v_exp_f32 v157, v157\n" \
  "v_exp_f32 v158, v158\n" \
  "v_exp_f32 v159, v159\n" \
  "v_cvt_pk_bf16_f32 v196, v152, v153\n" \
  "v_cvt_pk_bf16_f32 v197, v154, v155\n" \
  "v_cvt_pk_bf16_f32 v198, v156, v157\n" \
  "v_cvt_pk_bf16_f32 v199, v158, v159\n" \
  "ds_read_b128 v[200:203], %[pk0h] offset:31488\n" \
  "ds_read_b64 v[204:205], %[pk1h] offset:32512\n" \
  "v_mfma_f32_32x32x16_bf16 %[Ob], v[210:213], v[196:199], %[Ob]\n" \
  "ds_read_b128 v[206:209], %[pvh] offset:30144\n" \
  "ds_read_b128 v[210:213], %[pvh] offset:30816\n" \
  "v_exp_f32 v160, v160\n" \
  "v_exp_f32 v161, v161\n" \
  "v_exp_f32 v162, v162\n" \
  "v_exp_f32 v163, v163\n" \
  "v_exp_f32 v164, v164\n" \
  "v_exp_f32 v165, v165\n" \
  "v_exp_f32 v166, v166\n" \
  "v_exp_f32 v167, v167\n" \
  "v_cvt_pk_bf16_f32 v192, v160, v161\n" \
  "v_cvt_pk_bf16_f32 v193, v162, v163\n" \
  "v_cvt_pk_bf16_f32 v194, v164, v165\n" \
  "v_cvt_pk_bf16_f32 v195, v166, v167\n" \
  "s_waitcnt lgkmcnt(0)\n" \
  "v_mfma_f32_32x32x16_bf16 v[128:143], v[200:203], %[q0a], 0\n" \
  "v_exp_f32 v168, v168\n" \
  "v_exp_f32 v169, v169\n" \
  "v_mfma_f32_32x32x16_bf16 %[Oa], v[206:209], v[192:195], %[Oa]\n" \
  "v_exp_f32 v170, v170\n" \
  "v_exp_f32 v171, v171\n" \
  "v_exp_f32 v172, v172\n" \
  "v_mfma_f32_32x32x16_bf16 v[144:159], v[200:203], %[q0b], 0\n" \
  "v_exp_f32 v173, v173\n" \
  "v_exp_f32 v174, v174\n" \
  "v_exp_f32 v175, v175\n" \
  "v_cvt_pk_bf16_f32 v196, v168, v169\n" \
  "v_cvt_pk_bf16_f32 v197, v170, v171\n" \
  "v_cvt_pk_bf16_f32 v198, v172, v173\n" \
  "v_cvt_pk_bf16_f32 v199, v174, v175\n" \
  "v_exp_f32 v176, v176\n" \
  "v_exp_f32 v177, v177\n" \
  "v_mfma_f32_32x32x16_bf16 %[Oa], v[210:213], v[196:199], %[Oa]\n" \
  "v_exp_f32 v178, v178\n" \
  "v_exp_f32 v179, v179\n" \
  "v_mfma_f32_32x32x4_2b_bf16 v[128:159], v[204:205], %[q1], v[128:159]\n" \
  "v_exp_f32 v180, v180\n" \
  "v_exp_f32 v181, v181\n" \
  "v_exp_f32 v182, v182\n" \
  "v_exp_f32 v183, v183\n" \
  "v_cvt_pk_bf16_f32 v192, v176, v177\n" \
  "v_cvt_pk_bf16_f32 v193, v178, v179\n" \
  "v_cvt_pk_bf16_f32 v194, v180, v181\n" \
  "v_cvt_pk_bf16_f32 v195, v182, v183\n" \
  "v_exp_f32 v184, v184\n" \
  "v_exp_f32 v185, v185\n" \
  "v_mfma_f32_32x32x16_bf16 %[Ob], v[206:209], v[192:195], %[Ob]\n" \
  "v_exp_f32 v186, v186\n" \
  "v_exp_f32 v187, v187\n" \
  "v_exp_f32 v188, v188\n" \
  "v_exp_f32 v189, v189\n" \
  "v_exp_f32 v190, v190\n" \
  "v_exp_f32 v191, v191\n" \
  "v_cvt_pk_bf16_f32 v196, v184, v185\n" \
  "v_cvt_pk_bf16_f32 v197, v186, v187\n" \
  "v_cvt_pk_bf16_f32 v198, v188, v189\n" \
  "v_cvt_pk_bf16_f32 v199, v190, v191\n" \
  "ds_read_b128 v[200:203], %[pk0h] offset:34112\n" \
  "ds_read_b64 v[204:205], %[pk1h] offset:35136\n" \
  "v_mfma_f32_32x32x16_bf16 %[Ob], v[210:213], v[196:199], %[Ob]\n" \
  "ds_read_b128 v[206:209], %[pvh] offset:32768\n" \
  "ds_read_b128 v[210:213], %[pvh] offset:33440\n" \
  "v_exp_f32 v128, v128\n" \
  "v_exp_f32 v129, v129\n" \
  "v_exp_f32 v130, v130\n" \
  "v_exp_f32 v131, v131\n" \
  "v_exp_f32 v132, v132\n" \
  "v_exp_f32 v133, v133\n" \
  "v_exp_f32 v134, v134\n" \
  "v_exp_f32 v135, v135\n" \
  "v_cvt_pk_bf16_f32 v192, v128, v129\n" \
  "v_cvt_pk_bf16_f32 v193, v130, v131\n" \
  "v_cvt_pk_bf16_f32 v194, v132, v133\n" \
  "v_cvt_pk_bf16_f32 v195, v134, v135\n" \
  "s_waitcnt lgkmcnt(0)\n" \
  "v_mfma_f32_32x32x16_bf16 v[160:175], v[200:203], %[q0a], 0\n" \
  "v_exp_f32 v136, v136\n" \
  "v_exp_f32 v137, v137\n" \
  "v_mfma_f32_32x32x16_bf16 %[Oa], v[206:209], v[192:195], %[Oa]\n" \
  "v_exp_f32 v138, v138\n" \
  "v_exp_f32 v139, v139\n" \
  "v_exp_f32 v140, v140\n" \
  "v_mfma_f32_32x32x16_bf16 v[176:191], v[200:203], %[q0b], 0\n" \
  "v_exp_f32 v141, v141\n" \
  "v_exp_f32 v142, v142\n" \
  "v_exp_f32 v143, v143\n" \
  "v_cvt_pk_bf16_f32 v196, v136, v137\n" \
  "v_cvt_pk_bf16_f32 v197, v138, v139\n" \
  "v_cvt_pk_bf16_f32 v198, v140, v141\n" \
  "v_cvt_pk_bf16_f32 v199, v142, v143\n" \
  "v_exp_f32 v144, v144\n" \
  "v_exp_f32 v145, v145\n" \
  "v_mfma_f32_32x32x16_bf16 %[Oa], v[210:213], v[196:199], %[Oa]\n" \
  "v_exp_f32 v146, v146\n" \
  "v_exp_f32 v147, v147\n" \
  "v_mfma_f32_32x32x4_2b_bf16 v[160:191], v[204:205], %[q1], v[160:191]\n" \
  "v_exp_f32 v148, v148\n" \
  "v_exp_f32 v149, v149\n" \
  "v_exp_f32 v150, v150\n" \
  "v_exp_f32 v151, v151\n" \
  "v_cvt_pk_bf16_f32 v192, v144, v145\n" \
  "v_cvt_pk_bf16_f32 v193, v146, v147\n" \
  "v_cvt_pk_bf16_f32 v194, v148, v149\n" \
  "v_cvt_pk_bf16_f32 v195, v150, v151\n" \
  "v_exp_f32 v152, v152\n" \
  "v_exp_f32 v153, v153\n" \
  "v_mfma_f32_32x32x16_bf16 %[Ob], v[206:209], v[192:195], %[Ob]\n" \
  "v_exp_f32 v154, v154\n" \
  "v_exp_f32 v155, v155\n" \
  "v_exp_f32 v156, v156\n" \
  "v_exp_f32 v157, v157\n" \
  "v_exp_f32 v158, v158\n" \
  "v_exp_f32 v159, v159\n" \
  "v_cvt_pk_bf16_f32 v196, v152, v153\n" \
  "v_cvt_pk_bf16_f32 v197, v154, v155\n" \
  "v_cvt_pk_bf16_f32 v198, v156, v157\n" \
  "v_cvt_pk_bf16_f32 v199, v158, v159\n"
#define ATT_SEG3 \
  "ds_read_b128 v[200:203], %[pk0] offset:0\n" \
  "ds_read_b64 v[204:205], %[pk1] offset:1024\n" \
  "v_mfma_f32_32x32x16_bf16 %[Ob], v[210:213], v[196:199], %[Ob]\n" \
  "ds_read_b128 v[206:209], %[pvh] offset:35392\n" \
  "ds_read_b128 v[210:213], %[pvh] offset:36064\n" \
  "v_exp_f32 v160, v160\n" \
  "v_exp_f32 v161, v161\n" \
  "v_exp_f32 v162, v162\n" \
  "v_exp_f32 v163, v163\n" \
  "v_exp_f32 v164, v164\n" \
  "v_exp_f32 v165, v165\n" \
  "v_exp_f32 v166, v166\n" \
  "v_exp_f32 v167, v167\n" \
  "v_cvt_pk_bf16_f32 v192, v160, v161\n" \
  "v_cvt_pk_bf16_f32 v193, v162, v163\n" \
  "v_cvt_pk_bf16_f32 v194, v164, v165\n" \
  "v_cvt_pk_bf16_f32 v195, v166, v167\n" \
  "s_waitcnt lgkmcnt(0)\n" \
  "v_mfma_f32_32x32x16_bf16 v[128:143], v[200:203], %[q0a], 0\n" \
  "v_exp_f32 v168, v168\n" \
  "v_exp_f32 v169, v169\n" \
  "v_mfma_f32_32x32x16_bf16 %[Oa], v[206:209], v[192:195], %[Oa]\n" \
  "s_cmp_eq_u32 %[wpar], 0\n" \
  "s_cbranch_scc0 1f\n" \
  "s_mov_b32 m0, %[lw]\n" \
  "s_add_u32 %[lw], %[lw], 4096\n" \
  "buffer_load_dwordx4 %[vo], %[rs], %[sw] offen lds\n" \
  "buffer_load_dwordx4 %[vo], %[rs], %[sw] offen offset:1024 lds\n" \
  "buffer_load_dwordx4 %[vo], %[rs], %[sw] offen offset:2048 lds\n" \
  "buffer_load_dwordx4 %[vo], %[rs], %[sw] offen offset:3072 lds\n" \
  "s_add_u32 %[sw], %[sw], 4096\n" \
  "1:\n" \
  "v_exp_f32 v170, v170\n" \
  "v_exp_f32 v171, v171\n" \
  "v_exp_f32 v172, v172\n" \
  "v_mfma_f32_32x32x16_bf16 v[144:159], v[200:203], %[q0b], 0\n" \
  "v_exp_f32 v173, v173\n" \
  "v_exp_f32 v174, v174\n" \
  "v_exp_f32 v175, v175\n" \
  "v_cvt_pk_bf16_f32 v196, v168, v169\n" \
  "v_cvt_pk_bf16_f32 v197, v170, v171\n" \
  "v_cvt_pk_bf16_f32 v198, v172, v173\n" \
  "v_cvt_pk_bf16_f32 v199, v174, v175\n" \
  "v_exp_f32 v176, v176\n" \
  "v_exp_f32 v177, v177\n" \
  "v_mfma_f32_32x32x16_bf16 %[Oa], v[210:213], v[196:199], %[Oa]\n" \
  "v_exp_f32 v178, v178\n" \
  "v_exp_f32 v179, v179\n" \
  "v_mfma_f32_32x32x4_2b_bf16 v[128:159], v[204:205], %[q1], v[128:159]\n" \
  "v_exp_f32 v180, v180\n" \
  "v_exp_f32 v181, v181\n" \
  "v_exp_f32 v182, v182\n" \
  "v_exp_f32 v183, v183\n" \
  "v_cvt_pk_bf16_f32 v192, v176, v177\n" \
  "v_cvt_pk_bf16_f32 v193, v178, v179\n" \
  "v_cvt_pk_bf16_f32 v194, v180, v181\n" \
  "v_cvt_pk_bf16_f32 v195, v182, v183\n" \
  "v_exp_f32 v184, v184\n" \
  "v_exp_f32 v185, v185\n" \
  "v_mfma_f32_32x32x16_bf16 %[Ob], v[206:209], v[192:195], %[Ob]\n" \
  "s_cmp_eq_u32 %[wpar], 1\n" \
  "s_cbranch_scc0 1f\n" \
  "s_mov_b32 m0, %[lw]\n" \
  "s_add_u32 %[lw], %[lw], 4096\n" \
  "buffer_load_dwordx4 %[vo], %[rs], %[sw] offen lds\n" \
  "buffer_load_dwordx4 %[vo], %[rs], %[sw] offen offset:1024 lds\n" \
  "buffer_load_dwordx4 %[vo], %[rs], %[sw] offen offset:2048 lds\n" \
  "buffer_load_dwordx4 %[vo], %[rs], %[sw] offen offset:3072 lds\n" \
  "s_add_u32 %[sw], %[sw], 4096\n" \
  "1:\n" \
  "v_exp_f32 v186, v186\n" \
  "v_exp_f32 v187, v187\n" \
  "v_exp_f32 v188, v188\n" \
  "v_exp_f32 v189, v189\n" \
  "v_exp_f32 v190, v190\n" \
  "v_exp_f32 v191, v191\n" \
  "v_cvt_pk_bf16_f32 v196, v184, v185\n" \
  "v_cvt_pk_bf16_f32 v197, v186, v187\n" \
  "v_cvt_pk_bf16_f32 v198, v188, v189\n" \
  "v_cvt_pk_bf16_f32 v199, v190, v191\n" \
  "ds_read_b128 v[200:203], %[pk0] offset:2624\n" \
  "ds_read_b64 v[204:205], %[pk1] offset:3648\n" \
  "v_mfma_f32_32x32x16_bf16 %[Ob], v[210:213], v[196:199], %[Ob]\n" \
  "ds_read_b128 v[206:209], %[pv] offset:1280\n" \
  "ds_read_b128 v[210:213], %[pv] offset:1952\n" \
  "v_exp_f32 v128, v128\n" \
  "v_exp_f32 v129, v129\n" \
  "v_exp_f32 v130, v130\n" \
  "v_exp_f32 v131, v131\n" \
  "v_exp_f32 v132, v132\n" \
  "v_exp_f32 v133, v133\n" \
  "v_exp_f32 v134, v134\n" \
  "v_exp_f32 v135, v135\n" \
  "v_cvt_pk_bf16_f32 v192, v128, v129\n" \
  "v_cvt_pk_bf16_f32 v193, v130, v131\n" \
  "v_cvt_pk_bf16_f32 v194, v132, v133\n" \
  "v_cvt_pk_bf16_f32 v195, v134, v135\n" \
  "s_waitcnt lgkmcnt(0)\n" \
  "v_mfma_f32_32x32x16_bf16 v[160:175], v[200:203], %[q0a], 0\n" \
  "v_exp_f32 v136, v136\n" \
  "v_exp_f32 v137, v137\n" \
  "v_mfma_f32_32x32x16_bf16 %[Oa], v[206:209], v[192:195], %[Oa]\n" \
  "s_cmp_eq_u32 %[wpar], 0\n" \
  "s_cbranch_scc0 1f\n" \
  "s_mov_b32 m0, %[lw]\n" \
  "s_add_u32 %[lw], %[lw], 3072\n" \
  "buffer_load_dwordx4 %[vo], %[rs], %[sw] offen lds\n" \
  "buffer_load_dwordx4 %[vo], %[rs], %[sw] offen offset:1024 lds\n" \
  "buffer_load_dwordx4 %[vo], %[rs], %[sw] offen offset:2048 lds\n" \
  "s_add_u32 %[sw], %[sw], 3072\n" \
  "1:\n" \
  "v_exp_f32 v138, v138\n" \
  "v_exp_f32 v139, v139\n" \
  "v_exp_f32 v140, v140\n" \
  "v_mfma_f32_32x32x16_bf16 v[176:191], v[200:203], %[q0b], 0\n" \
  "v_exp_f32 v141, v141\n" \
  "v_exp_f32 v142, v142\n" \
  "v_exp_f32 v143, v143\n" \
  "v_cvt_pk_bf16_f32 v196, v136, v137\n" \
  "v_cvt_pk_bf16_f32 v197, v138, v139\n" \
  "v_cvt_pk_bf16_f32 v198, v140, v141\n" \
  "v_cvt_pk_bf16_f32 v199, v142, v143\n" \
  "v_exp_f32 v144, v144\n" \
  "v_exp_f32 v145, v145\n" \
  "v_mfma_f32_32x32x16_bf16 %[Oa], v[210:213], v[196:199], %[Oa]\n" \
  "v_exp_f32 v146, v146\n" \
  "v_exp_f32 v147, v147\n" \
  "v_mfma_f32_32x32x4_2b_bf16 v[160:191], v[204:205], %[q1], v[160:191]\n" \
  "v_exp_f32 v148, v148\n" \
  "v_exp_f32 v149, v149\n" \
  "v_exp_f32 v150, v150\n" \
  "v_exp_f32 v151, v151\n" \
  "v_cvt_pk_bf16_f32 v192, v144, v145\n" \
  "v_cvt_pk_bf16_f32 v193, v146, v147\n" \
  "v_cvt_pk_bf16_f32 v194, v148, v149\n" \
  "v_cvt_pk_bf16_f32 v195, v150, v151\n" \
  "v_exp_f32 v152, v152\n" \
  "v_exp_f32 v153, v153\n" \
  "v_mfma_f32_32x32x16_bf16 %[Ob], v[206:209], v[192:195], %[Ob]\n" \
  "s_cmp_eq_u32 %[wpar], 1\n" \
  "s_cbranch_scc0 1f\n" \
  "s_mov_b32 m0, %[lw]\n" \
  "s_add_u32 %[lw], %[lw], 3072\n" \
  "buffer_load_dwordx4 %[vo], %[rs], %[sw] offen lds\n" \
  "buffer_load_dwordx4 %[vo], %[rs], %[sw] offen offset:1024 lds\n" \
  "buffer_load_dwordx4 %[vo], %[rs], %[sw] offen offset:2048 lds\n" \
  "s_add_u32 %[sw], %[sw], 3072\n" \
  "1:\n" \
  "v_exp_f32 v154, v154\n" \
  "v_exp_f32 v155, v155\n" \
  "v_exp_f32 v156, v156\n" \
  "v_exp_f32 v157, v157\n" \
  "v_exp_f32 v158, v158\n" \
  "v_exp_f32 v159, v159\n" \
  "v_cvt_pk_bf16_f32 v196, v152, v153\n" \
  "v_cvt_pk_bf16_f32 v197, v154, v155\n" \
  "v_cvt_pk_bf16_f32 v198, v156, v157\n" \
  "v_cvt_pk_bf16_f32 v199, v158, v159\n" \
  "ds_read_b128 v[200:203], %[pk0] offset:5248\n" \
  "ds_read_b64 v[204:205], %[pk1] offset:6272\n" \
  "v_mfma_f32_32x32x16_bf16 %[Ob], v[210:213], v[196:199], %[Ob]\n" \
  "ds_read_b128 v[206:209], %[pv] offset:3904\n" \
  "ds_read_b128 v[210:213], %[pv] offset:4576\n" \
  "v_exp_f32 v160, v160\n" \
  "v_exp_f32 v161, v161\n" \
  "v_exp_f32 v162, v162\n" \
  "v_exp_f32 v163, v163\n" \
  "v_exp_f32 v164, v164\n" \
  "v_exp_f32 v165, v165\n" \
  "v_exp_f32 v166, v166\n" \
  "v_exp_f32 v167, v167\n" \
  "v_cvt_pk_bf16_f32 v192, v160, v161\n" \
  "v_cvt_pk_bf16_f32 v193, v162, v163\n" \
  "v_cvt_pk_bf16_f32 v194, v164, v165\n" \
  "v_cvt_pk_bf16_f32 v195, v166, v167\n" \
  "s_waitcnt lgkmcnt(0)\n" \
  "v_mfma_f32_32x32x16_bf16 v[128:143], v[200:203], %[q0a], 0\n" \
  "v_exp_f32 v168, v168\n" \
  "v_exp_f32 v169, v169\n" \
  "v_mfma_f32_32x32x16_bf16 %[Oa], v[206:209], v[192:195], %[Oa]\n" \
  "s_cmp_eq_u32 %[wpar], 0\n" \
  "s_cbranch_scc0 1f\n" \
  "s_mov_b32 m0, %[lw]\n" \
  "s_add_u32 %[lw], %[lw], 3072\n" \
  "buffer_load_dwordx4 %[vo], %[rs], %[sw] offen lds\n" \
  "buffer_load_dwordx4 %[vo], %[rs], %[sw] offen offset:1024 lds\n" \
  "buffer_load_dwordx4 %[vo], %[rs], %[sw] offen offset:2048 lds\n" \
  "s_add_u32 %[sw], %[sw], 3072\n" \
  "1:\n" \
  "v_exp_f32 v170, v170\n" \
  "v_exp_f32 v171, v171\n" \
  "v_exp_f32 v172, v172\n" \
  "v_mfma_f32_32x32x16_bf16 v[144:159], v[200:203], %[q0b], 0\n" \
  "v_exp_f32 v173, v173\n" \
  "v_exp_f32 v174, v174\n" \
  "v_exp_f32 v175, v175\n" \
  "v_cvt_pk_bf16_f32 v196, v168, v169\n" \
  "v_cvt_pk_bf16_f32 v197, v170, v171\n" \
  "v_cvt_pk_bf16_f32 v198, v172, v173\n" \
  "v_cvt_pk_bf16_f32 v199, v174, v175\n" \
  "v_exp_f32 v176, v176\n" \
  "v_exp_f32 v177, v177\n" \
  "v_mfma_f32_32x32x16_bf16 %[Oa], v[210:213], v[196:199], %[Oa]\n" \
  "v_exp_f32 v178, v178\n" \
  "v_exp_f32 v179, v179\n" \
  "v_mfma_f32_32x32x4_2b_bf16 v[128:159], v[204:205], %[q1], v[128:159]\n" \
  "v_exp_f32 v180, v180\n" \
  "v_exp_f32 v181, v181\n" \
  "v_exp_f32 v182, v182\n" \
  "v_exp_f32 v183, v183\n" \
  "v_cvt_pk_bf16_f32 v192, v176, v177\n" \
  "v_cvt_pk_bf16_f32 v193, v178, v179\n" \
  "v_cvt_pk_bf16_f32 v194, v180, v181\n" \
  "v_cvt_pk_bf16_f32 v195, v182, v183\n" \
  "v_exp_f32 v184, v184\n" \
  "v_exp_f32 v185, v185\n" \
  "v_mfma_f32_32x32x16_bf16 %[Ob], v[206:209], v[192:195], %[Ob]\n" \
  "s_cmp_eq_u32 %[wpar], 1\n" \
  "s_cbranch_scc0 1f\n" \
  "s_mov_b32 m0, %[lw]\n" \
  "s_add_u32 %[lw], %[lw], 3072\n" \
  "buffer_load_dwordx4 %[vo], %[rs], %[sw] offen lds\n" \
  "buffer_load_dwordx4 %[vo], %[rs], %[sw] offen offset:1024 lds\n" \
  "buffer_load_dwordx4 %[vo], %[rs], %[sw] offen offset:2048 lds\n" \
  "s_add_u32 %[sw], %[sw], 3072\n" \
  "1:\n" \
  "v_exp_f32 v186, v186\n" \
  "v_exp_f32 v187, v187\n" \
  "v_exp_f32 v188, v188\n" \
  "v_exp_f32 v189, v189\n" \
  "v_exp_f32 v190, v190\n" \
  "v_exp_f32 v191, v191\n" \
  "v_cvt_pk_bf16_f32 v196, v184, v185\n" \
  "v_cvt_pk_bf16_f32 v197, v186, v187\n" \
  "v_cvt_pk_bf16_f32 v198, v188, v189\n" \
  "v_cvt_pk_bf16_f32 v199, v190, v191\n" \
  "ds_read_b128 v[200:203], %[pk0] offset:7872\n" \
  "ds_read_b64 v[204:205], %[pk1] offset:8896\n" \
  "v_mfma_f32_32x32x16_bf16 %[Ob], v[210:213], v[196:199], %[Ob]\n" \
  "ds_read_b128 v[206:209], %[pv] offset:6528\n" \
  "ds_read_b128 v[210:213], %[pv] offset:7200\n" \
  "v_exp_f32 v128, v128\n" \
  "v_exp_f32 v129, v129\n" \
  "v_exp_f32 v130, v130\n" \
  "v_exp_f32 v131, v131\n" \
  "v_exp_f32 v132, v132\n" \
  "v_exp_f32 v133, v133\n" \
  "v_exp_f32 v134, v134\n" \
  "v_exp_f32 v135, v135\n" \
  "v_cvt_pk_bf16_f32 v192, v128, v129\n" \
  "v_cvt_pk_bf16_f32 v193, v130, v131\n" \
  "v_cvt_pk_bf16_f32 v194, v132, v133\n" \
  "v_cvt_pk_bf16_f32 v195, v134, v135\n" \
  "s_waitcnt lgkmcnt(0)\n" \
  "v_mfma_f32_32x32x16_bf16 v[160:175], v[200:203], %[q0a], 0\n" \
  "v_exp_f32 v136, v136\n" \
  "v_exp_f32 v137, v137\n" \
  "v_mfma_f32_32x32x16_bf16 %[Oa], v[206:209], v[192:195], %[Oa]\n" \
  "s_cmp_eq_u32 %[wpar], 0\n" \
  "s_cbranch_scc0 1f\n" \
  "s_mov_b32 m0, %[lw]\n" \
  "s_add_u32 %[lw], %[lw], 3072\n" \
  "buffer_load_dwordx4 %[vo], %[rs], %[sw] offen lds\n" \
  "buffer_load_dwordx4 %[vo], %[rs], %[sw] offen offset:1024 lds\n" \
  "buffer_load_dwordx4 %[vo], %[rs], %[sw] offen offset:2048 lds\n" \
  "s_add_u32 %[sw], %[sw], 3072\n" \
  "1:\n" \
  "v_exp_f32 v138, v138\n" \
  "v_exp_f32 v139, v139\n" \
  "v_exp_f32 v140, v140\n" \
  "v_mfma_f32_32x32x16_bf16 v[176:191], v[200:203], %[q0b], 0\n" \
  "v_exp_f32 v141, v141\n" \
  "v_exp_f32 v142, v142\n" \
  "v_exp_f32 v143, v143\n" \
  "v_cvt_pk_bf16_f32 v196, v136, v137\n" \
  "v_cvt_pk_bf16_f32 v197, v138, v139\n" \
  "v_cvt_pk_bf16_f32 v198, v140, v141\n" \
  "v_cvt_pk_bf16_f32 v199, v142, v143\n" \
  "v_exp_f32 v144, v144\n" \
  "v_exp_f32 v145, v145\n" \
  "v_mfma_f32_32x32x16_bf16 %[Oa], v[210:213], v[196:199], %[Oa]\n" \
  "v_exp_f32 v146, v146\n" \
  "v_exp_f32 v147, v147\n" \
  "v_mfma_f32_32x32x4_2b_bf16 v[160:191], v[204:205], %[q1], v[160:191]\n" \
  "v_exp_f32 v148, v148\n" \
  "v_exp_f32 v149, v149\n" \
  "v_exp_f32 v150, v150\n" \
  "v_exp_f32 v151, v151\n" \
  "v_cvt_pk_bf16_f32 v192, v144, v145\n" \
  "v_cvt_pk_bf16_f32 v193, v146, v147\n" \
  "v_cvt_pk_bf16_f32 v194, v148, v149\n" \
  "v_cvt_pk_bf16_f32 v195, v150, v151\n" \
  "v_exp_f32 v152, v152\n" \
  "v_exp_f32 v153, v153\n" \
  "v_mfma_f32_32x32x16_bf16 %[Ob], v[206:209], v[192:195], %[Ob]\n" \
  "s_cmp_eq_u32 %[wpar], 1\n" \
  "s_cbranch_scc0 1f\n" \
  "s_mov_b32 m0, %[lw]\n" \
  "s_add_u32 %[lw], %[lw], 3072\n" \
  "buffer_load_dwordx4 %[vo], %[rs], %[sw] offen lds\n" \
  "buffer_load_dwordx4 %[vo], %[rs], %[sw] offen offset:1024 lds\n" \
  "buffer_load_dwordx4 %[vo], %[rs], %[sw] offen offset:2048 lds\n" \
  "s_add_u32 %[sw], %[sw], 3072\n" \
  "1:\n" \
  "v_exp_f32 v154, v154\n" \
  "v_exp_f32 v155, v155\n" \
  "v_exp_f32 v156, v156\n" \
  "v_exp_f32 v157, v157\n" \
  "v_exp_f32 v158, v158\n" \
  "v_exp_f32 v159, v159\n" \
  "v_cvt_pk_bf16_f32 v196, v152, v153\n" \
  "v_cvt_pk_bf16_f32 v197, v154, v155\n" \
  "v_cvt_pk_bf16_f32 v198, v156, v157\n" \
  "v_cvt_pk_bf16_f32 v199, v158, v159\n" \
  "s_nop 1\n" \
  "v_mfma_f32_32x32x16_bf16 %[Ob], v[210:213], v[196:199], %[Ob]\n" \
  "ds_read_b128 v[206:209], %[pv] offset:9152\n" \
  "ds_read_b128 v[210:213], %[pv] offset:9824\n" \
  "v_exp_f32 v160, v160\n" \
  "v_exp_f32 v161, v161\n" \
  "v_exp_f32 v162, v162\n" \
  "v_exp_f32 v163, v163\n" \
  "v_exp_f32 v164, v164\n" \
  "v_exp_f32 v165, v165\n" \
  "v_exp_f32 v166, v166\n" \
  "v_exp_f32 v167, v167\n" \
  "v_cvt_pk_bf16_f32 v192, v160, v161\n" \
  "v_cvt_pk_bf16_f32 v193, v162, v163\n" \
  "v_cvt_pk_bf16_f32 v194, v164, v165\n" \
  "v_cvt_pk_bf16_f32 v195, v166, v167\n" \
  "s_waitcnt lgkmcnt(0)\n" \
  "v_exp_f32 v168, v168\n" \
  "v_exp_f32 v169, v169\n" \
  "v_mfma_f32_32x32x16_bf16 %[Oa], v[206:209], v[192:195], %[Oa]\n" \
  "v_exp_f32 v170, v170\n" \
  "v_exp_f32 v171, v171\n" \
  "v_exp_f32 v172, v172\n" \
  "v_exp_f32 v173, v173\n" \
  "v_exp_f32 v174, v174\n" \
  "v_exp_f32 v175, v175\n" \
  "v_cvt_pk_bf16_f32 v196, v168, v169\n" \
  "v_cvt_pk_bf16_f32 v197, v170, v171\n" \
  "v_cvt_pk_bf16_f32 v198, v172, v173\n" \
  "v_cvt_pk_bf16_f32 v199, v174, v175\n" \
  "v_exp_f32 v176, v176\n" \
  "v_exp_f32 v177, v177\n" \
  "v_mfma_f32_32x32x16_bf16 %[Oa], v[210:213], v[196:199], %[Oa]\n" \
  "v_exp_f32 v178, v178\n" \
  "v_exp_f32 v179, v179\n" \
  "v_exp_f32 v180, v180\n" \
  "v_exp_f32 v181, v181\n" \
  "v_exp_f32 v182, v182\n" \
  "v_exp_f32 v183, v183\n" \
  "v_cvt_pk_bf16_f32 v192, v176, v177\n" \
  "v_cvt_pk_bf16_f32 v193, v178, v179\n" \
  "v_cvt_pk_bf16_f32 v194, v180, v181\n" \
  "v_cvt_pk_bf16_f32 v195, v182, v183\n" \
  "v_exp_f32 v184, v184\n" \
  "v_exp_f32 v185, v185\n" \
  "v_mfma_f32_32x32x16_bf16 %[Ob], v[206:209], v[192:195], %[Ob]\n" \
  "v_exp_f32 v186, v186\n" \
  "v_exp_f32 v187, v187\n" \
  "v_exp_f32 v188, v188\n" \
  "v_exp_f32 v189, v189\n" \
  "v_exp_f32 v190, v190\n" \
  "v_exp_f32 v191, v191\n" \
  "v_cvt_pk_bf16_f32 v196, v184, v185\n" \
  "v_cvt_pk_bf16_f32 v197, v186, v187\n" \
  "v_cvt_pk_bf16_f32 v198, v188, v189\n" \
  "v_cvt_pk_bf16_f32 v199, v190, v191\n" \
  "s_nop 1\n" \
  "v_mfma_f32_32x32x16_bf16 %[Ob], v[210:213], v[196:199], %[Ob]\n" \
  "s_nop 15\n"

constexpr int LDS_BYTES = 163840;
constexpr int L_QX = 0;
constexpr int L_WO = 13312;
constexpr int L_OXA = 64512, L_OXB = 138240 - 4 * 3072;
constexpr int L_PX = 64512;
constexpr int L_S5 = 71680;
constexpr int L_MX = 0;
constexpr int L_WQ0 = 92160, L_WQ1 = 0;
constexpr int W2_GRP = 51200;
constexpr int L_KX = 61440;
constexpr int L_SX = 65536;
constexpr int L_CSQ = 153600;
constexpr int L_CS1 = L_CSQ + 512;
constexpr int L_B1 = L_CS1 + 1024;
constexpr int L_FLG = 157184;
constexpr int L_TOUCH = 157696;
constexpr int L_LN = L_B1 + 1024;

struct Ctx {
  rsrc_t rs;
  const char* ws;
  LAS char* lds;
  unsigned lds0;
  int g, j, w, mb, kh, lane, tid, q4, tk;
  int role;
};

DI Ctx fresh(const Ctx& o) {
  Ctx c = o;
  int g = o.g, j = o.j, w = o.w, lane = o.lane;
  const char* ws = o.ws;
  asm volatile("" : "+s"(g), "+s"(j), "+s"(w), "+v"(lane), "+s"(ws));
  c.g = g; c.j = j; c.w = w; c.lane = lane; c.ws = ws;
  c.mb = w & 3; c.kh = w >> 2; c.q4 = lane >> 4; c.tk = lane & 15; c.tid = w * 64 + lane;
  c.role = w == 3 ? 7 : w == 7 ? 3 : w;
  return c;
}
DI gu32* cnt_ptr(const Ctx& c, int layer, int hop, int g) { return (gu32*)(c.ws + OFF_FLAGS + ((layer * 5 + hop) * 32 + g) * FLAG_STRIDE); }
DI void drain_bar() { WAIT_VM_BAR(0); }
DI void signal(const Ctx& c, int layer, int hop) { if (c.tid == 0) __hip_atomic_fetch_add(cnt_ptr(c, layer, hop, c.g), 1u, RLX_AGENT); }
DI void spin_fail(const Ctx& c) { __hip_atomic_store((gu32*)(c.ws + OFF_TMO), 1u, RLX_AGENT); }
DI void poll(const Ctx& c, gu32* ptr, unsigned want, int role = 7) {
  if (c.role == role) {
    for (unsigned spins = 0;; ++spins) {
      const unsigned v = __hip_atomic_load(ptr, RLX_AGENT);
      if (__all(v >= want)) break;
      if (spins > SPIN_CAP) { if (c.lane == 0) spin_fail(c); break; }
      __builtin_amdgcn_s_sleep(1);
    }
  }
  __builtin_amdgcn_fence(__ATOMIC_ACQUIRE, "wavefront");
}
DI void wait_cnt(const Ctx& c, int layer, int hop, int role = 7) { poll(c, cnt_ptr(c, layer, hop, c.g), 8u, role); }
DI gu32* kvc_ptr(const Ctx& c, int layer, int rep, int shard) { return (gu32*)(c.ws + OFF_KVC + ((layer * 8 + rep) * 8 + shard) * FLAG_STRIDE); }
DI void signal_kv(const Ctx& c, int layer) { if (c.tid < 8) __hip_atomic_fetch_add(kvc_ptr(c, layer, c.tid, c.g & 7), 1u, RLX_AGENT); }
DI void wait_kv(const Ctx& c, int layer) { poll(c, kvc_ptr(c, layer, blockIdx.x & 7, c.lane & 7), 32u); }

template <int AUX> DI void load_act(const Ctx& c, unsigned base, u32x4 (&af)[10]) {
#pragma unroll
  for (int pp = 0; pp < 2; ++pp) {
    const unsigned o = base + (4 * c.kh + 2 * pp) * ACT_SL;
    af[5 * pp + 0] = ld128<AUX>(c.rs, c.lane * 16, o);
    af[5 * pp + 1] = ld128<AUX>(c.rs, c.lane * 16, o + 1024);
    af[5 * pp + 2] = ld128<AUX>(c.rs, c.lane * 16, o + ACT_SL);
    af[5 * pp + 3] = ld128<AUX>(c.rs, c.lane * 16, o + ACT_SL + 1024);
    const u32x2 h0 = ld64<AUX>(c.rs, c.lane * 8, o + 2048), h1 = ld64<AUX>(c.rs, c.lane * 8, o + ACT_SL + 2048);
    af[5 * pp + 4] = u32x4{h0[0], h0[1], h1[0], h1[1]};
  }
}
struct QFrag { u32x4 q0a, q0b; u32x2 q1; };
template <int NS, int NB, int STRIDE, int S0, int NA, class Hook>
DI void gemm_lds(LAS const char* wl, u32x4 (&af)[NA], f32x4 (&acc)[NB], Hook hook) {
  u32x4 w[2][NB];
#pragma unroll
  for (int nb = 0; nb < NB; ++nb) w[0][nb] = lds128(wl + nb * 1024);
#pragma unroll
  for (int s = 0; s < NS; ++s) {
    hook(S0 + s);
    SB();
    if (s + 1 < NS) {
#pragma unroll
      for (int nb = 0; nb < NB; ++nb) w[(s + 1) & 1][nb] = lds128(wl + (s + 1) * STRIDE + nb * 1024);
    }
#pragma unroll
    for (int nb = 0; nb < NB; ++nb) acc[nb] = mfma16(w[s & 1][nb], af[S0 + s], acc[nb]);
#pragma unroll
    for (int nb = 0; nb < NB; ++nb) { __builtin_amdgcn_sched_group_barrier(0x8, 1, 0); __builtin_amdgcn_sched_group_barrier(0x100, 1, 0); }
    SB();
  }
}
struct NoHook { DI void operator()(int) const {} };

DI void lds_wait(LAS char* word, unsigned want) {
  while (__hip_atomic_load((LAS unsigned*)word, __ATOMIC_RELAXED, __HIP_MEMORY_SCOPE_WORKGROUP) < want) __builtin_amdgcn_s_sleep(1);
  asm volatile("" ::: "memory");
}
DI int mlp1_block(const Ctx& c) {
  const int nblk = c.j < 6 ? 7 : 6;
  return (c.j < 6 ? 7 * c.j : 42 + 6 * (c.j - 6)) + (c.role < nblk ? c.role : nblk - 1);
}
DI rsrc_t w1_rsrc(const Ctx& c) { return __builtin_amdgcn_make_buffer_rsrc((void*)c.ws, 0, c.role < (c.j < 6 ? 7 : 6) ? WS_USED : 0, 0x00020000); }
template <int I0, int I1> DI void load_w1(const Ctx& c, u32x4 (&wf)[40]) {
  const rsrc_t rw = w1_rsrc(c);
  const unsigned w1o = OFF_W1 + mlp1_block(c) * W1_B;
#pragma unroll
  for (int i = I0; i < I1; ++i) wf[i] = ld128<0>(rw, c.lane * 16, w1o + i * 1024);
}
struct Consts { float cs1[4], b1, csq[4]; };
DI Consts request_constants(const Ctx& c) {
  Consts k;
  const int blk = mlp1_block(c), l32 = c.lane & 31, t96 = c.tid < 96 ? c.tid : 0;
#pragma unroll
  for (int pp = 0; pp < 4; ++pp) {
    k.cs1[pp] = __uint_as_float(__builtin_amdgcn_raw_buffer_load_b32(c.rs, l32 * 4, OFF_CS1 + (pp * FF + blk * 32) * 4, 0));
    k.csq[pp] = __uint_as_float(__builtin_amdgcn_raw_buffer_load_b32(c.rs, t96 * 4, OFF_CSQ + (pp * 8 + c.j) * 384, 0));
  }
  k.b1 = __uint_as_float(__builtin_amdgcn_raw_buffer_load_b32(c.rs, l32 * 4, OFF_B1F + blk * 128, 0));
  return k;
}
DI void commit_constants(const Ctx& c, const Consts& k) {
  if (c.lane < 32) {
    *(LAS float*)(c.lds + L_CS1 + c.w * 128 + c.lane * 4) = (k.cs1[0] + k.cs1[1]) + (k.cs1[2] + k.cs1[3]);
    *(LAS float*)(c.lds + L_B1 + c.w * 128 + c.lane * 4) = k.b1 * LOG2E;
  }
  if (c.tid < 4) *(LAS unsigned*)(c.lds + L_FLG + c.tid * 4) = 0u;
  if (c.tid < 96) *(LAS float*)(c.lds + L_CSQ + c.tid * 4) = (k.csq[0] + k.csq[1]) + (k.csq[2] + k.csq[3]);
}

template <int KH> DI void issue_wq(const Ctx& c0) {
  const Ctx c = fresh(c0);
  const int p0 = c.w < 7 ? 8 * c.w : 52;
  dma_run<8, false>(c.ws + OFF_WQ + c.j * WQ_J + KH * 61440 + p0 * 1024, c.lane * 16, c.lds0 + (KH ? L_WQ1 : L_WQ0) + p0 * 1024);
}
DI QFrag phase_q(const Ctx& c0, int layer, unsigned xoff) {
  const Ctx c = fresh(c0);
  const unsigned lbase = OFF_LAYER + layer * LAYER_BYTES;
  u32x4 af[10];
  if (layer == 0) {
    const Consts k = request_constants(c);
    load_act<0>(c, xoff + c.g * ACT_G + c.mb * ACT_MB, af);
    issue_wq<0>(c);
    issue_wq<1>(c);
    commit_constants(c, k);
    WAIT_VM_BAR(0);
  } else {
    wait_cnt(c, layer - 1, HOP_X2);
    WAIT_VM_BAR(0);
    load_act<SC1>(c, xoff + c.g * ACT_G + c.mb * ACT_MB, af);
  }
  LAS const char* wl = c.lds + (c.kh ? L_WQ1 : L_WQ0) + c.lane * 16;
  f32x4 akv;
  float s1, s2;
  {
    u32x4 wk[10];
#pragma unroll
    for (int s = 0; s < 10; ++s) wk[s] = lds128(wl + 51200 + s * 1024);
    SB();
    akv = f32x4{0.f, 0.f, 0.f, 0.f};
    f32x4 sx = {0.f, 0.f, 0.f, 0.f}, sq = {0.f, 0.f, 0.f, 0.f};
    const u32x4 ones = {ONES2, ONES2, ONES2, ONES2};
    if (c.j < 3) {
#pragma unroll
      for (int s = 0; s < 10; ++s) { akv = mfma16(wk[s], af[s], akv); sx = mfma16(ones, af[s], sx); sq = mfma16(af[s], af[s], sq); }
    } else {
#pragma unroll
      for (int s = 0; s < 10; ++s) { akv = mfma16(af[s], wk[s], akv); sx = mfma16(ones, af[s], sx); sq = mfma16(af[s], af[s], sq); }
    }
    s1 = sx[0];
    s2 = diag_bcast(sq, c.lane);
  }
  if (c.kh == 1) *(LAS f32x4*)(c.lds + L_KX + c.mb * 1024 + c.lane * 16) = akv;
  if (c.q4 == 0) *(LAS f32x2*)(c.lds + L_SX + c.w * 128 + c.tk * 8) = f32x2{s1, s2};
  BAR();
  float mean, rstd;
  { const f32x2 o = *(LAS const f32x2*)(c.lds + L_SX + (c.w ^ 4) * 128 + c.tk * 8); ln_stats(s1 + o[0], s2 + o[1], mean, rstd); }
  float m4[4], r4[4];
#pragma unroll
  for (int i = 0; i < 4; ++i) { m4[i] = __shfl(mean, 4 * c.q4 + i); r4[i] = __shfl(rstd, 4 * c.q4 + i); }
  if (c.kh == 0) {
    const f32x4 a = akv + lds128f(c.lds + L_KX + c.mb * 1024 + c.lane * 16);
    const unsigned kvt = lbase + L_KV + (2 * c.g + (c.mb >> 1)) * TILEB;
    if (c.j < 3) {
      const f32x4 cs = lds128f(c.lds + L_CSQ + 5 * 64 + c.q4 * 16);
      const u32x2 v = {cvtpk((a[0] - mean * cs[0]) * rstd, (a[1] - mean * cs[1]) * rstd), cvtpk((a[2] - mean * cs[2]) * rstd, (a[3] - mean * cs[3]) * rstd)};
      const int key = 16 * (c.mb & 1) + c.tk;
      const unsigned o = c.j < 2 ? (key + 32 * c.j) * 16 + c.q4 * 8 : K1OFF + key * 8;
      if (c.q4 < (c.j < 2 ? 2 : 1)) st64<SC1>(c.rs, v, o, kvt);
    } else {
      const float csv = *(LAS const float*)(c.lds + L_CSQ + 5 * 64 + c.tk * 4);
      const u32x2 v = {cvtpk((a[0] - m4[0] * csv) * r4[0], (a[1] - m4[1] * csv) * r4[1]), cvtpk((a[2] - m4[2] * csv) * r4[2], (a[3] - m4[3] * csv) * r4[3])};
      const int d = 4 * (c.j - 3) + c.tk;
      if (c.tk < 4) st64<SC1>(c.rs, v, V0OFF + (c.mb & 1) * VSTEP + ((c.q4 & 1) * 21 + d) * 16 + (c.q4 >> 1) * 8, kvt);
    }
  }
  f32x4 acc[5];
#pragma unroll
  for (int nb = 0; nb < 5; ++nb) acc[nb] = f32x4{0.f, 0.f, 0.f, 0.f};
  gemm_lds<10, 5, 5120, 0>(wl, af, acc, NoHook());
  drain_bar();
  signal_kv(c, layer);
#pragma unroll
  for (int nb = 0; nb < 5; ++nb) *(LAS f32x4*)(c.lds + L_QX + c.w * 5120 + nb * 1024 + c.lane * 16) = acc[nb];
  BAR();
  const float rq = rstd * QSCALE;
  unsigned qw[10];
#pragma unroll
  for (int nb = 0; nb < 5; ++nb) {
    const f32x4 a = acc[nb] + lds128f(c.lds + L_QX + (c.w ^ 4) * 5120 + nb * 1024 + c.lane * 16);
    const f32x4 cs = lds128f(c.lds + L_CSQ + nb * 64 + c.q4 * 16);
    qw[2 * nb] = cvtpk((a[0] - mean * cs[0]) * rq, (a[1] - mean * cs[1]) * rq);
    qw[2 * nb + 1] = cvtpk((a[2] - mean * cs[2]) * rq, (a[3] - mean * cs[3]) * rq);
  }
  QFrag q;
  q.q0a = u32x4{qw[0], qw[1], qw[2], qw[3]};
  q.q0b = u32x4{qw[4], qw[5], qw[6], qw[7]};
  q.q1 = u32x2{qw[8], qw[9]};
  asm volatile("" : "+v"(q.q0a), "+v"(q.q0b), "+v"(q.q1));
  return q;
}

DI void attn_fallback(const Ctx& c, unsigned kvbase, u32x4 q0, u32x2 q1s, f32x16& O) {
  const int r = c.lane & 31, h = c.lane >> 5;
  const u32x4 qx = h == 0 ? u32x4{q1s[0], q1s[1], 0u, 0u} : u32x4{0u, 0u, 0u, 0u};
  const f32x16 z = {};
  float m = -INFINITY;
#pragma unroll 1
  for (int t = 0; t < 64; ++t) {
    const unsigned o = kvbase + t * TILEB;
    const u32x4 k0 = ld128<SC1>(c.rs, c.lane * 16, o);
    const u32x2 k1 = ld64<SC1>(c.rs, r * 8, o + K1OFF);
    f32x16 s = mfma32(k0, q0, z);
    s = mfma32(u32x4{k1[0], k1[1], 0u, 0u}, qx, s);
#pragma unroll
    for (int i = 0; i < 16; ++i) m = fmaxf(m, s[i]);
  }
  {
    auto sw = __builtin_amdgcn_permlane32_swap(__float_as_uint(m), __float_as_uint(m), false, false);
    m = fmaxf(__uint_as_float(sw[0]), __uint_as_float(sw[1]));
  }
  O = z;
  const unsigned vo = V0OFF + (h * 21 + (r < 20 ? r : 20)) * 16;
#pragma unroll 1
  for (int t = 0; t < 64; ++t) {
    const unsigned o = kvbase + t * TILEB;
    const u32x4 k0 = ld128<SC1>(c.rs, c.lane * 16, o);
    const u32x2 k1 = ld64<SC1>(c.rs, r * 8, o + K1OFF);
    const u32x4 v0 = ld128<SC1>(c.rs, vo, o), v1 = ld128<SC1>(c.rs, vo + VSTEP, o);
    f32x16 s = mfma32(k0, q0, z);
    s = mfma32(u32x4{k1[0], k1[1], 0u, 0u}, qx, s);
    unsigned pw[8];
#pragma unroll
    for (int i = 0; i < 8; ++i) pw[i] = cvtpk(__builtin_amdgcn_exp2f(s[2 * i] - m), __builtin_amdgcn_exp2f(s[2 * i + 1] - m));
    O = mfma32(v0, u32x4{pw[0], pw[1], pw[2], pw[3]}, O);
    O = mfma32(v1, u32x4{pw[4], pw[5], pw[6], pw[7]}, O);
  }
}

constexpr int W1_EARLY = 24, W1_MID = 32;
DI void phase_attn(const Ctx& c0, int layer, const QFrag& q, u32x4 (&wf)[40]) {
  const Ctx c = fresh(c0);
  const unsigned lbase = OFF_LAYER + layer * LAYER_BYTES;
  const char* kv = c.ws + lbase + L_KV;
  wait_kv(c, layer);
  BAR();
  const int hf = c.w & 1, wi = c.w >> 1;
  const char* gh = kv + hf * 32 * TILEB;
  const unsigned lh = c.lds0 + hf * HALFB;
#pragma unroll
  for (int r = 0; r < 2; ++r) dma<3, true>(gh + (4 * r + wi) * 3072, c.lane * 16, lh + (4 * r + wi) * 3072);
  const int r = c.lane & 31, h = c.lane >> 5;
  const unsigned hb = c.lds0 + c.kh * HALFB;
  const unsigned pk0 = hb + c.lane * 16, pk1 = hb + r * 8, pv = hb + (h * 21 + (r < 20 ? r : 20)) * 16;
  const unsigned pk0h = pk0 + 14 * TILEB, pk1h = pk1 + 14 * TILEB, pvh = pv + 14 * TILEB, vo = c.lane * 16;
  f32x16 Oa = {}, Ob = {};
  f32x32 S0, S1; f32x16 T0; f32x8 T1;
  unsigned sv;
#define ATT_IN [q0a] "v"(q.q0a), [q0b] "v"(q.q0b), [q1] "v"(q.q1), [pk0] "v"(pk0), [pk1] "v"(pk1), [pv] "v"(pv), [pk0h] "v"(pk0h), [pk1h] "v"(pk1h), [pvh] "v"(pvh), [vo] "v"(vo)
  {
    unsigned lk = lh + (8 + wi) * 3072, sk = lbase + L_KV + hf * 32 * TILEB + (8 + wi) * 3072;
    asm volatile(
        "s_mov_b32 %[sv], m0\n"
        "s_waitcnt vmcnt(3)\n s_barrier\n"
        ATT_PRO
        ATT_SEG0
        "s_waitcnt vmcnt(9)\n s_barrier\n"
        ATT_SEG1
        "s_waitcnt vmcnt(0)\n s_barrier\n"
        "s_mov_b32 m0, %[sv]\n"
        : [Oa] "+v"(Oa), [Ob] "+v"(Ob), "=&{v[128:159]}"(S0), "=&{v[160:191]}"(S1), "=&{v[192:207]}"(T0), "=&{v[208:215]}"(T1),
          [sv] "=&s"(sv), [lk] "+s"(lk), [sk] "+s"(sk)
        : ATT_IN, [rs] "s"(c.rs) : "memory", "scc");
  }
  {
    const int p2 = 8 + wi < 10 ? 8 + wi : 10;
    const char* gl = gh + 28 * TILEB;
    dma<1, true>(gl + wi * 1024, c.lane * 16, lh + wi * 1024);
    dma<1, true>(gl + wi * 1024 + 4096, c.lane * 16, lh + wi * 1024 + 4096);
    dma<1, true>(gl + p2 * 1024, c.lane * 16, lh + p2 * 1024);
  }
  f32x32 W0, W1, W2;
  {
    const rsrc_t rw = w1_rsrc(c);
    unsigned so = OFF_W1 + mlp1_block(c) * W1_B;
    const int wm3 = c.w % 3;
    asm volatile(
        ATT_SEG2
        "s_waitcnt vmcnt(0)\n s_barrier\n"
        : [Oa] "+v"(Oa), [Ob] "+v"(Ob), "+{v[128:159]}"(S0), "+{v[160:191]}"(S1), "+{v[192:207]}"(T0), "+{v[208:215]}"(T1),
          "=&{v[32:63]}"(W0), "=&{v[64:95]}"(W1), "=&{v[96:127]}"(W2), [so] "+s"(so)
        : ATT_IN, [rw] "s"(rw), [wm3] "s"(wm3) : "memory", "scc");
  }
#pragma unroll
  for (int i = 0; i < 8; ++i) {
    wf[i] = __builtin_bit_cast(u32x4, f32x4{W0[4 * i], W0[4 * i + 1], W0[4 * i + 2], W0[4 * i + 3]});
    wf[8 + i] = __builtin_bit_cast(u32x4, f32x4{W1[4 * i], W1[4 * i + 1], W1[4 * i + 2], W1[4 * i + 3]});
    wf[16 + i] = __builtin_bit_cast(u32x4, f32x4{W2[4 * i], W2[4 * i + 1], W2[4 * i + 2], W2[4 * i + 3]});
  }
  {
    const int p0 = wi < 3 ? 13 * wi : 37;
    unsigned lw = lh + L_WO + p0 * 1024, sw = OFF_WO + c.j * WO_J + hf * 51200 + p0 * 1024;
    const int wpar = (c.w >> 1) & 1;
    asm volatile(
        "s_mov_b32 %[sv], m0\n"
        ATT_SEG3
        "s_mov_b32 m0, %[sv]\n"
        "s_barrier\n"
        : [Oa] "+v"(Oa), [Ob] "+v"(Ob), "+{v[128:159]}"(S0), "+{v[160:191]}"(S1), "+{v[192:207]}"(T0), "+{v[208:215]}"(T1),
          [sv] "=&s"(sv), [lw] "+s"(lw), [sw] "+s"(sw)
        : ATT_IN, [rs] "s"(c.rs), [wpar] "s"(wpar) : "memory", "scc");
  }
#undef ATT_IN
  {
    LAS char* xw = c.lds + (c.w < 4 ? L_OXA : L_OXB) + c.w * 3072 + c.lane * 16;
    const f32x16& snd = c.kh == 0 ? Ob : Oa;
#pragma unroll
    for (int i = 0; i < 3; ++i) *(LAS f32x4*)(xw + i * 1024) = f32x4{snd[4 * i], snd[4 * i + 1], snd[4 * i + 2], snd[4 * i + 3]};
  }
  BAR();
  f32x16 O = c.kh == 0 ? Oa : Ob;
  {
    LAS const char* xr = c.lds + (c.w >= 4 ? L_OXA : L_OXB) + (c.w ^ 4) * 3072 + c.lane * 16;
#pragma unroll
    for (int i = 0; i < 3; ++i) {
      const f32x4 t = lds128f(xr + i * 1024);
      O[4 * i] += t[0]; O[4 * i + 1] += t[1]; O[4 * i + 2] += t[2]; O[4 * i + 3] += t[3];
    }
  }
  float l;
  { auto sw = __builtin_amdgcn_permlane32_swap(__float_as_uint(O[8]), __float_as_uint(O[8]), false, false); l = __uint_as_float(sw[1]); }
  {
    bool ok = l >= 0x1p-90f && l <= 0x1p90f;
    float amax = 0.f;
#pragma unroll
    for (int i = 0; i < 8; ++i) amax = fmaxf(amax, fabsf(O[i]));
    if (h == 0) {
#pragma unroll
      for (int i = 8; i < 12; ++i) amax = fmaxf(amax, fabsf(O[i]));
    }
    float asum = 0.f;
#pragma unroll
    for (int i = 0; i < 8; ++i) asum += O[i];
    ok = ok && amax < INFINITY && asum == asum;
    if (!__all(ok)) {
      u32x2 q1s;
      q1s[0] = __shfl(q.q1[0], (c.kh * 32 + c.lane) & 63);
      q1s[1] = __shfl(q.q1[1], (c.kh * 32 + c.lane) & 63);
      attn_fallback(c, lbase + L_KV, c.kh == 0 ? q.q0a : q.q0b, q1s, O);
      auto sw = __builtin_amdgcn_permlane32_swap(__float_as_uint(O[8]), __float_as_uint(O[8]), false, false);
      l = __uint_as_float(sw[1]);
    }
  }
  const float il = __builtin_amdgcn_rcpf(l);
  const unsigned ob = lbase + L_O + c.g * ACT_G + c.mb * ACT_MB + c.j * ACT_SL;
  st128<SC1>(c.rs, u32x4{cvtpk(O[0] * il, O[1] * il), cvtpk(O[2] * il, O[3] * il), cvtpk(O[4] * il, O[5] * il), cvtpk(O[6] * il, O[7] * il)},
             c.lane * 16, ob + c.kh * 1024);
  if (h == 0) st64<SC1>(c.rs, u32x2{cvtpk(O[8] * il, O[9] * il), cvtpk(O[10] * il, O[11] * il)}, c.lane * 8, ob + 2048 + c.kh * 256);
  drain_bar();
  signal(c, layer, HOP_O);
}

struct Resid { u32x4 f; u32x2 h; };
template <int AUX> DI Resid load_resid(const Ctx& c, unsigned actoff) {
  Resid r;
  const unsigned o = actoff + c.g * ACT_G + c.mb * ACT_MB + c.j * ACT_SL;
  r.f = ld128<AUX>(c.rs, c.lane * 16, o + c.kh * 1024);
  r.h = ld64<AUX>(c.rs, c.lane * 8, o + 2048);
  return r;
}
DI void epilogue640(const Ctx& c, f32x4 (&acc)[5], int lx, const Resid& res, unsigned actoff, u16* out) {
  LAS char* xb = c.lds + lx + c.mb * 5120 + c.lane * 16;
  if (c.kh == 0) { *(LAS f32x4*)(xb + 2048) = acc[2]; *(LAS f32x4*)(xb + 3072) = acc[3]; }
  else { *(LAS f32x4*)(xb) = acc[0]; *(LAS f32x4*)(xb + 1024) = acc[1]; *(LAS f32x4*)(xb + 4096) = acc[4]; }
  BAR();
  f32x4 a, b2, d = {0.f, 0.f, 0.f, 0.f};
  if (c.kh == 0) { a = acc[0] + lds128f(xb); b2 = acc[1] + lds128f(xb + 1024); d = acc[4] + lds128f(xb + 4096); }
  else { a = acc[2] + lds128f(xb + 2048); b2 = acc[3] + lds128f(xb + 3072); }
  a[0] += bflo(res.f[0]); a[1] += bfhi(res.f[0]); a[2] += bflo(res.f[1]); a[3] += bfhi(res.f[1]);
  b2[0] += bflo(res.f[2]); b2[1] += bfhi(res.f[2]); b2[2] += bflo(res.f[3]); b2[3] += bfhi(res.f[3]);
  const u32x4 pf = {cvtpk(a[0], a[1]), cvtpk(a[2], a[3]), cvtpk(b2[0], b2[1]), cvtpk(b2[2], b2[3])};
  u32x2 ph = {0u, 0u};
  if (c.kh == 0) {
    d[0] += bflo(res.h[0]); d[1] += bfhi(res.h[0]); d[2] += bflo(res.h[1]); d[3] += bfhi(res.h[1]);
    ph = u32x2{cvtpk(d[0], d[1]), cvtpk(d[2], d[3])};
  }
  if (out) {
    u16* o = out + (size_t)(c.g * 64 + c.mb * 16 + c.tk) * DM + 80 * c.j + 4 * c.q4;
    *(u32x2*)(o + 32 * c.kh) = u32x2{pf[0], pf[1]};
    *(u32x2*)(o + 32 * c.kh + 16) = u32x2{pf[2], pf[3]};
    if (c.kh == 0) *(u32x2*)(o + 64) = ph;
  } else {
    const unsigned o = actoff + c.g * ACT_G + c.mb * ACT_MB + c.j * ACT_SL;
    st128<SC1>(c.rs, pf, c.lane * 16, o + c.kh * 1024);
    if (c.kh == 0) st64<SC1>(c.rs, ph, c.lane * 8, o + 2048);
  }
}

DI void phase_oproj(const Ctx& c0, int layer, unsigned xoff, u32x4 (&wf)[40]) {
  const Ctx c = fresh(c0);
  const unsigned lbase = OFF_LAYER + layer * LAYER_BYTES;
  const Resid res = load_resid<SC1>(c, xoff);
  wait_cnt(c, layer, HOP_O);
  BAR();
  u32x4 af[10];
  load_act<SC1>(c, lbase + L_O + c.g * ACT_G + c.mb * ACT_MB, af);
  f32x4 acc[5];
#pragma unroll
  for (int nb = 0; nb < 5; ++nb) acc[nb] = f32x4{0.f, 0.f, 0.f, 0.f};
  LAS const char* wl = c.lds + c.kh * HALFB + L_WO + c.lane * 16;
  gemm_lds<10, 5, 5120, 0>(wl, af, acc, NoHook());
  epilogue640(c, acc, L_PX, res, lbase + L_X1, nullptr);
  drain_bar();
  signal(c, layer, HOP_X1);
  load_w1<W1_EARLY, W1_MID>(c, wf);
}

template <int NCH> DI void issue_w2(const Ctx& c, int chunk0, int ldsoff) {
  constexpr int NP = (NCH * 10 + 7) / 8;
  int p0 = c.w * NP; if (p0 + NP > NCH * 10) p0 = NCH * 10 - NP;
  dma_run<NP, false>(c.ws + OFF_W2 + c.j * W2_J + chunk0 * 10240 + p0 * 1024, c.lane * 16, c.lds0 + ldsoff + p0 * 1024);
}
DI float mish(float y) {
  const float e = __builtin_amdgcn_exp2f(fminf(y, 43.f));
  const float n = e * __builtin_fmaf(e, LN2, 2.f * LN2);
  return y * (n * __builtin_amdgcn_rcpf(__builtin_fmaf(n, LOG2E, 2.f)));
}
DI void phase_mlp1(const Ctx& c0, int layer, u32x4 (&wf)[40]) {
  const Ctx c = fresh(c0);
  const unsigned lbase = OFF_LAYER + layer * LAYER_BYTES;
  const bool active = c.role < (c.j < 6 ? 7 : 6);
  const int blk = mlp1_block(c);
  wait_cnt(c, layer, HOP_X1);
  BAR();
  dma_run<10, true>(c.ws + lbase + L_X1 + c.g * ACT_G + c.w * 10240, c.lane * 16, c.lds0 + c.w * 10240);
  WAIT_VM_BAR(0);
  issue_w2<5>(c, 0, 2 * W2_GRP);
  f32x4 acc[2][4];
#pragma unroll
  for (int nb = 0; nb < 2; ++nb)
#pragma unroll
    for (int m = 0; m < 4; ++m) acc[nb][m] = f32x4{0.f, 0.f, 0.f, 0.f};
  {
    LAS const char* x16 = c.lds + c.lane * 16;
    LAS const char* x8 = c.lds + c.lane * 8;
    u32x4 a[3][4];
    auto rd = [&](int ks, u32x4 (&dst)[4]) {
      const int pp = ks / 5, sub = ks % 5;
#pragma unroll
      for (int m = 0; m < 4; ++m) {
        if (sub < 4) dst[m] = lds128(x16 + m * ACT_MB + (2 * pp + (sub >> 1)) * ACT_SL + (sub & 1) * 1024);
        else {
          const u32x2 h0 = lds64(x8 + m * ACT_MB + (2 * pp) * ACT_SL + 2048), h1 = lds64(x8 + m * ACT_MB + (2 * pp + 1) * ACT_SL + 2048);
          dst[m] = u32x4{h0[0], h0[1], h1[0], h1[1]};
        }
      }
    };
    rd(0, a[0]); rd(1, a[1]);
    if (active) {
#pragma unroll
      for (int ks = 0; ks < 20; ++ks) {
        if (ks == 4) load_w1<W1_MID, 40>(c, wf);
        SB();
        if (ks + 2 < 20) rd(ks + 2, a[(ks + 2) % 3]);
#pragma unroll
        for (int m = 0; m < 4; ++m) {
          acc[0][m] = mfma16(wf[2 * ks], a[ks % 3][m], acc[0][m]);
          acc[1][m] = mfma16(wf[2 * ks + 1], a[ks % 3][m], acc[1][m]);
        }
#pragma unroll
        for (int m = 0; m < 8; ++m) { __builtin_amdgcn_sched_group_barrier(0x8, 1, 0); __builtin_amdgcn_sched_group_barrier(0x100, 1, 0); }
        SB();
      }
      asm volatile("s_waitcnt lgkmcnt(0)" ::: "memory");
      if (c.lane == 0) __hip_atomic_fetch_add((LAS unsigned*)(c.lds + L_FLG + 4), 1u, __ATOMIC_RELAXED, __HIP_MEMORY_SCOPE_WORKGROUP);
      lds_wait(c.lds + L_FLG, layer + 1);
    } else if (c.role == 7) {
      __builtin_amdgcn_s_setprio(2);
      const u32x4 ones = {ONES2, ONES2, ONES2, ONES2};
#pragma unroll
      for (int ks = 0; ks < 20; ++ks) {
        if (ks + 2 < 20) rd(ks + 2, a[(ks + 2) % 3]);
#pragma unroll
        for (int m = 0; m < 4; ++m) {
          acc[0][m] = mfma16(ones, a[ks % 3][m], acc[0][m]);
          acc[1][m] = mfma16(a[ks % 3][m], a[ks % 3][m], acc[1][m]);
        }
#pragma unroll
        for (int m = 0; m < 8; ++m) { __builtin_amdgcn_sched_group_barrier(0x8, 1, 0); __builtin_amdgcn_sched_group_barrier(0x100, 1, 0); }
        SB();
      }
#pragma unroll
      for (int m = 0; m < 4; ++m) {
        float mean, rstd;
        ln_stats(acc[0][m][0], diag_bcast(acc[1][m], c.lane), mean, rstd);
        const float rl = rstd * LOG2E;
        if (c.q4 == 0) *(LAS f32x2*)(c.lds + L_LN + m * 128 + c.tk * 8) = f32x2{rl, mean * rl};
      }
      asm volatile("s_waitcnt lgkmcnt(0)" ::: "memory");
      if (c.lane == 0) __hip_atomic_store((LAS unsigned*)(c.lds + L_FLG), (unsigned)(layer + 1), __ATOMIC_RELAXED, __HIP_MEMORY_SCOPE_WORKGROUP);
      __builtin_amdgcn_s_setprio(0);
      {
#pragma unroll
        for (int i = 0; i < 4; ++i) touch8k(c.rs, c.lane * 128, OFF_W2 + c.j * W2_J + 15 * 10240 + ((c.g & 3) * 4 + i) * 7680, c.lds0 + L_TOUCH);
      }
      lds_wait(c.lds + L_FLG + 4, (c.j < 6 ? 7u : 6u) * (layer + 1));
      dma_run<50, false>(c.ws + OFF_W2 + c.j * W2_J + 5 * 10240, c.lane * 16, c.lds0);
    }
  }
  if (active) {
    f32x4 cs[2], bi[2];
#pragma unroll
    for (int nb = 0; nb < 2; ++nb) {
      cs[nb] = lds128f(c.lds + L_CS1 + c.w * 128 + nb * 64 + c.q4 * 16);
      bi[nb] = lds128f(c.lds + L_B1 + c.w * 128 + nb * 64 + c.q4 * 16);
    }
#pragma unroll
    for (int m = 0; m < 4; ++m) {
      const f32x2 ln = *(LAS const f32x2*)(c.lds + L_LN + m * 128 + c.tk * 8);
      unsigned pw[4];
#pragma unroll
      for (int nb = 0; nb < 2; ++nb) {
        float y[4];
#pragma unroll
        for (int i = 0; i < 4; ++i) y[i] = mish(__builtin_fmaf(acc[nb][m][i], ln[0], __builtin_fmaf(-ln[1], cs[nb][i], bi[nb][i])));
        pw[2 * nb] = cvtpk(y[0], y[1]); pw[2 * nb + 1] = cvtpk(y[2], y[3]);
      }
      st128<SC1>(c.rs, u32x4{pw[0], pw[1], pw[2], pw[3]}, c.lane * 16, lbase + L_H + (c.g * 4 + m) * H_MB + blk * 1024);
    }
  }
  if (c.role == 7) BAR(); else drain_bar();
  signal(c, layer, HOP_H);
  issue_w2<5>(c, 10, W2_GRP);
}

#define H_UP 6
#define H_STEP 2
DI void phase_mlp2(const Ctx& c0, int layer, u16* out) {
  const Ctx c = fresh(c0);
  const unsigned lbase = OFF_LAYER + layer * LAYER_BYTES;
  const Resid res = load_resid<SC1>(c, lbase + L_X1);
  wait_cnt(c, layer, HOP_H);
  WAIT_VM_BAR(0);
  u32x4 af[27];
  const unsigned hb = lbase + L_H + (c.g * 4 + c.mb) * H_MB + 27 * c.kh * 1024;
  const int rot = mlp2_rot(c.j);
  auto load_h = [&](int s) { const int bb = s + rot < 27 ? s + rot : s + rot - 27; af[s] = ld128<SC1>(c.rs, c.lane * 16, hb + bb * 1024); };
#pragma unroll
  for (int s = 0; s < H_UP; ++s) load_h(s);
  auto hook = [&](int s) {
#pragma unroll
    for (int i = H_UP + H_STEP * s; i < H_UP + H_STEP * (s + 1); ++i) if (i < 27) load_h(i);
  };
  f32x4 acc[5];
#pragma unroll
  for (int nb = 0; nb < 5; ++nb) acc[nb] = f32x4{0.f, 0.f, 0.f, 0.f};
  LAS const char* wl = c.lds + c.kh * 5120 + c.lane * 16;
#define MLP2_STAGE(k, n, off) gemm_lds<n, 5, 10240, 5 * (k)>(wl + (off), af, acc, hook);
  MLP2_STAGE(0, 5, 2 * W2_GRP)
  MLP2_STAGE(1, 5, 0)
  BAR(); issue_w2<5>(c, 15, 2 * W2_GRP);
  MLP2_STAGE(2, 5, W2_GRP)
  WAIT_VM_BAR(0); issue_w2<5>(c, 20, 0); issue_w2<2>(c, 25, L_S5);
  MLP2_STAGE(3, 5, 2 * W2_GRP)
  WAIT_VM_BAR(3);
  if (layer == 0) issue_wq<0>(c);
  MLP2_STAGE(4, 5, 0)
  if (layer == 0) WAIT_VM_BAR(8); else WAIT_VM_BAR(0);
  MLP2_STAGE(5, 2, L_S5)
#undef MLP2_STAGE
  epilogue640(c, acc, L_MX, res, lbase + L_X2, out);
}

__global__ __launch_bounds__(512, 2) void main_kernel(Params p) {
  extern __shared__ __attribute__((aligned(16))) char lds_[];
  asm volatile("mish_main_code_begin:");
  Ctx c;
  c.tid = threadIdx.x; c.lane = c.tid & 63; c.q4 = c.lane >> 4; c.tk = c.lane & 15;
  c.w = __builtin_amdgcn_readfirstlane(c.tid >> 6); c.mb = c.w & 3; c.kh = c.w >> 2; c.role = 0;
  const int b = blockIdx.x;
  c.g = (b & 7) * 4 + (b >> 6); c.j = (b >> 3) & 7;
  c.ws = p.ws; c.lds = (LAS char*)lds_; c.lds0 = (unsigned)(uintptr_t)lds_;
  c.rs = __builtin_amdgcn_make_buffer_rsrc((void*)p.ws, 0, WS_USED, 0x00020000);
#pragma unroll 1
  for (int layer = 0; layer < 2; ++layer) {
    const QFrag q = phase_q(c, layer, layer == 0 ? OFF_XT : OFF_LAYER + L_X2);
    u32x4 wf[40];
    phase_attn(c, layer, q, wf);
    phase_oproj(c, layer, layer == 0 ? OFF_XT : OFF_LAYER + L_X2, wf);
    phase_mlp1(c, layer, wf);
    phase_mlp2(c, layer, layer == 1 ? p.out : nullptr);
    if (layer == 0) { drain_bar(); signal(c, layer, HOP_X2); issue_wq<1>(c); }
  }
  asm volatile("mish_main_code_end:");
}

}

extern "C" void kernel_launch(void* const* d_in, const int* in_sizes, int n_in, void* d_out, int out_size, void* d_ws, size_t ws_size,
                              hipStream_t stream) {
  Params p;
  p.x = (const u16*)d_in[0]; p.wq = (const u16*)d_in[1]; p.wk = (const u16*)d_in[2]; p.wv = (const u16*)d_in[3]; p.wo = (const u16*)d_in[4];
  p.w1 = (const u16*)d_in[6]; p.b1 = (const u16*)d_in[7]; p.w2 = (const u16*)d_in[8];
  p.ws = (char*)d_ws; p.out = (u16*)d_out;
  (void)hipFuncSetAttribute((const void*)main_kernel, hipFuncAttributeMaxDynamicSharedMemorySize, LDS_BYTES);
  hipLaunchKernelGGL(prep_kernel, dim3(PJ_END), dim3(256), PREP_LDS, stream, p);
  hipLaunchKernelGGL(main_kernel, dim3(256), dim3(512), LDS_BYTES, stream, p);
}
```
